```python
import torch

def _softmax(x, axis=-1):
    return torch.softmax(x, dim=axis)
def _gelu(x):
    return torch.nn.functional.gelu(x, approximate="tanh")
def _silu(x):
    return torch.nn.functional.silu(x)
def _sigmoid(x):
    return torch.sigmoid(x)
def _layernorm(x, axis=-1, eps=1e-5):
    m = x.mean(dim=axis, keepdim=True)
    v = x.var(dim=axis, keepdim=True, unbiased=False)
    return (x - m) / torch.sqrt(v + eps)
def _scatter_add(tgt, idx, src, axis=0):
    return tgt.clone().index_add_(axis, idx, src)
def _segment_sum(src, seg, n):
    out = torch.zeros((n,) + src.shape[1:], dtype=src.dtype)
    return out.index_add_(0, seg, src)
def _pad(x, pw):
    flat = tuple(v for pair in reversed(pw) for v in pair)
    return torch.nn.functional.pad(x, flat)

kernel_name = 'recurrent_scan__grouped_kv_mqa__dense_activation_relu__prompt'

def setup_inputs():

    _w = (lambda a: (lambda t: t.to(torch.bfloat16) if t.is_floating_point() else t)(torch.as_tensor(a)))
    def _A_setup():
        B, S, D, H = 1, 2048, 512, 32
        Dh = D // H
        return {
            "x": _w((lambda *s: torch.randn(*s, dtype=torch.bfloat16) * 0.05)(B, S, D)),
            "wq": _w((lambda *s: torch.randn(*s, dtype=torch.bfloat16) * 0.05)(D, H * Dh)),
            "wk": _w((lambda *s: torch.randn(*s, dtype=torch.bfloat16) * 0.05)(D, Dh)),
            "wv": _w((lambda *s: torch.randn(*s, dtype=torch.bfloat16) * 0.05)(D, Dh)),
            "wo": _w((lambda *s: torch.randn(*s, dtype=torch.bfloat16) * 0.05)(H * Dh, D)),
        }
    _A_in = _A_setup()

    def _B_setup():
        B, S, D, Dff = 1, 2048, 512, 1792
        return {
            "x": _w((lambda *s: torch.randn(*s, dtype=torch.bfloat16) * 0.05)(B, S, D)),
            "w1": _w((lambda *s: torch.randn(*s, dtype=torch.bfloat16) * 0.05)(D, Dff)),
            "w2": _w((lambda *s: torch.randn(*s, dtype=torch.bfloat16) * 0.05)(Dff, D)),
        }
    _B_in = _B_setup()

    return {
        "A_x": _A_in["x"],
        "A_wq": _A_in["wq"],
        "A_wk": _A_in["wk"],
        "A_wv": _A_in["wv"],
        "A_wo": _A_in["wo"],
        "B_x": _B_in["x"],
        "B_w1": _B_in["w1"],
        "B_w2": _B_in["w2"],
    }

def reference(A_x, A_wq, A_wk, A_wv, A_wo, B_x, B_w1, B_w2):
    def _A_ref(x, wq, wk, wv, wo):
        B, S, D = x.shape
        H = 32
        Dh = D // H
        q = (lambda x, *d: x.permute(*d))((x @ wq).reshape(B, S, H, Dh), 0, 2, 1, 3)
        k = x @ wk
        v = x @ wv
        scores = torch.einsum("bhqd,bkd->bhqk", q, k) * (Dh ** -0.5)
        p = _softmax(scores, axis=-1)
        o = torch.einsum("bhqk,bkd->bhqd", p, v)
        return (lambda x, *d: x.permute(*d))(o, 0, 2, 1, 3).reshape(B, S, H * Dh) @ wo

    def _B_ref(x, w1, w2):
        h = torch.einsum("bsd,df->bsf", x, w1)
        zero = torch.zeros_like(h)
        h = torch.maximum(h, zero)
        return torch.einsum("bsf,fd->bsd", h, w2)

    x = A_x
    xn = _layernorm(x, axis=-1)
    s = _A_ref(xn, A_wq, A_wk, A_wv, A_wo)
    g = _silu(_B_ref(xn, B_w1, B_w2))
    return x + s * g
```

```cpp
#include <hip/hip_runtime.h>
#include <hip/hip_bf16.h>
typedef unsigned short u16;
__device__ __forceinline__ float b2f(u16 v) { return __uint_as_float((unsigned)v << 16); }
__device__ __forceinline__ u16 f2b(float f) { unsigned u = __float_as_uint(f); u += 0x7FFF + ((u >> 16) & 1); return u >> 16; }
__device__ __forceinline__ float rb(float f) { return b2f(f2b(f)); }
__global__ void k_ln(const u16* x, u16* xn) {
    int row = blockIdx.x, l = threadIdx.x; float v[8]; float s = 0;
    for (int i = 0; i < 8; ++i) { v[i] = b2f(x[row * 512 + l * 8 + i]); s += v[i]; }
    for (int o = 32; o; o >>= 1) s += __shfl_xor(s, o);
    float m = s / 512, q = 0;
    for (int i = 0; i < 8; ++i) q += (v[i] - m) * (v[i] - m);
    for (int o = 32; o; o >>= 1) q += __shfl_xor(q, o);
    float mb = rb(m), var = rb(q / 512), den = rb(sqrtf(rb(var + 1e-5f)));
    for (int i = 0; i < 8; ++i) xn[row * 512 + l * 8 + i] = f2b(rb(v[i] - mb) / den);
}
template<int RELU> __global__ void k_gemm(const u16* A, const u16* B, u16* C, int M, int N, int K) {
    int n = blockIdx.x * blockDim.x + threadIdx.x, m = blockIdx.y; if (n >= N) return;
    float s = 0; for (int k = 0; k < K; ++k) s += b2f(A[(size_t)m * K + k]) * b2f(B[(size_t)k * N + n]);
    if (RELU) s = fmaxf(s, 0.f);
    C[(size_t)m * N + n] = f2b(s);
}
__global__ void k_attn(const u16* q, const u16* k, const u16* v, u16* o) {
    int idx = blockIdx.x * blockDim.x + threadIdx.x;
    float qq[16], acc[16]; for (int d = 0; d < 16; ++d) { qq[d] = b2f(q[idx * 16 + d]) * 0.25f; acc[d] = 0; }
    float m = -1e30f, l = 0;
    for (int j = 0; j < 2048; ++j) { float s = 0; for (int d = 0; d < 16; ++d) s += qq[d] * b2f(k[j * 16 + d]);
        float mn = fmaxf(m, s), a = __expf(m - mn), p = __expf(s - mn); l = l * a + p; m = mn;
        for (int d = 0; d < 16; ++d) acc[d] = acc[d] * a + p * b2f(v[j * 16 + d]); }
    for (int d = 0; d < 16; ++d) o[idx * 16 + d] = f2b(acc[d] / l);
}
__global__ void k_fin(const u16* x, const u16* s, const u16* g, u16* out) {
    int i = blockIdx.x * blockDim.x + threadIdx.x; float gg = b2f(g[i]); float sg = rb(gg / (1.f + __expf(-gg)));
    out[i] = f2b(b2f(x[i]) + rb(b2f(s[i]) * sg));
}
extern "C" void kernel_launch(void* const* d_in, const int* in_sizes, int n_in, void* d_out, int out_size, void* d_ws, size_t ws_size, hipStream_t stream) {
    const u16 *x = (const u16*)d_in[0], *wq = (const u16*)d_in[1], *wk = (const u16*)d_in[2], *wv = (const u16*)d_in[3], *wo = (const u16*)d_in[4], *w1 = (const u16*)d_in[6], *w2 = (const u16*)d_in[7];
    u16* ws = (u16*)d_ws; u16 *xn = ws, *q = xn + 2048 * 512, *k = q + 2048 * 512, *v = k + 2048 * 16, *o = v + 2048 * 16, *s = o + 2048 * 512, *h = s + 2048 * 512, *g = h + 2048 * 1792;
    k_ln<<<2048, 64, 0, stream>>>(x, xn);
    k_gemm<0><<<dim3(2, 2048), 256, 0, stream>>>(xn, wq, q, 2048, 512, 512);
    k_gemm<0><<<dim3(1, 2048), 64, 0, stream>>>(xn, wk, k, 2048, 16, 512);
    k_gemm<0><<<dim3(1, 2048), 64, 0, stream>>>(xn, wv, v, 2048, 16, 512);
    k_attn<<<65536 / 64, 64, 0, stream>>>(q, k, v, o);
    k_gemm<0><<<dim3(2, 2048), 256, 0, stream>>>(o, wo, s, 2048, 512, 512);
    k_gemm<1><<<dim3(7, 2048), 256, 0, stream>>>(xn, w1, h, 2048, 1792, 512);
    k_gemm<0><<<dim3(2, 2048), 256, 0, stream>>>(h, w2, g, 2048, 512, 1792);
    k_fin<<<2048 * 512 / 256, 256, 0, stream>>>(x, s, g, (u16*)d_out);
}
```

```cpp
#include <hip/hip_runtime.h>
#include <hip/hip_bf16.h>

typedef unsigned short u16;
typedef unsigned int u32;
typedef unsigned long long u64;
typedef u32 u32x2 __attribute__((ext_vector_type(2)));
typedef u32 u32x4 __attribute__((ext_vector_type(4)));
typedef u32 u32x16 __attribute__((ext_vector_type(16)));
typedef u32 u32x32 __attribute__((ext_vector_type(32)));
typedef float f32x2 __attribute__((ext_vector_type(2)));
typedef float f32x4 __attribute__((ext_vector_type(4)));
typedef float f32x8 __attribute__((ext_vector_type(8)));
typedef float f32x16 __attribute__((ext_vector_type(16)));
typedef float f32x32 __attribute__((ext_vector_type(32)));
typedef __bf16 bf16x2 __attribute__((ext_vector_type(2)));
typedef __bf16 bf16x8 __attribute__((ext_vector_type(8)));
typedef __bf16 bf16x16 __attribute__((ext_vector_type(16)));
typedef short s16x4 __attribute__((ext_vector_type(4)));
#define AS3 __attribute__((address_space(3)))
#define AS1 __attribute__((address_space(1)))
#define RFL(x) __builtin_amdgcn_readfirstlane(x)
#define MEMBAR() asm volatile("" ::: "memory")

#define ASM_TAGI_A \
    "buffer_load_dwordx4 v[128:131], %1, %2, %3 offen offset:0 sc1\n\t" \
    "buffer_load_dwordx4 v[132:135], %1, %2, %3 offen offset:1024 sc1\n\t" \
    "buffer_load_dwordx4 v[136:139], %1, %2, %3 offen offset:2048 sc1\n\t" \
    "buffer_load_dwordx4 v[140:143], %1, %2, %3 offen offset:3072 sc1\n\t"
#define ASM_TAGTRY_A \
    "s_waitcnt vmcnt(0)\n\t" \
    "v_min3_u32 v160, v129, v131, v133\n\t" \
    "v_min3_u32 v160, v160, v135, v137\n\t" \
    "v_min3_u32 v160, v160, v139, v141\n\t" \
    "v_min_u32 v160, v160, v143\n\t" \
    "v_max3_u32 v161, v129, v131, v133\n\t" \
    "v_max3_u32 v161, v161, v135, v137\n\t" \
    "v_max3_u32 v161, v161, v139, v141\n\t" \
    "v_max_u32 v161, v161, v143\n\t" \
    "v_cmp_eq_u32 vcc, %4, v160\n\t" \
    "v_cmp_eq_u32 s[64:65], %4, v161\n\t" \
    "s_nop 0\n\t" \
    "s_and_b64 vcc, vcc, s[64:65]\n\t" \
    "s_cmp_eq_u64 vcc, -1\n\t" \
    "s_cbranch_scc1 1f\n\t" \
    "buffer_load_dwordx4 v[128:131], %1, %2, %3 offen offset:0 sc1\n\t" \
    "buffer_load_dwordx4 v[132:135], %1, %2, %3 offen offset:1024 sc1\n\t" \
    "buffer_load_dwordx4 v[136:139], %1, %2, %3 offen offset:2048 sc1\n\t" \
    "buffer_load_dwordx4 v[140:143], %1, %2, %3 offen offset:3072 sc1\n\t" \
    "1:\n\t"
#define ASM_TAGSPIN_A \
    "s_mov_b32 s66, 0\n\t" \
    "1:\n\t" \
    "s_waitcnt vmcnt(0)\n\t" \
    "v_min3_u32 v160, v129, v131, v133\n\t" \
    "v_min3_u32 v160, v160, v135, v137\n\t" \
    "v_min3_u32 v160, v160, v139, v141\n\t" \
    "v_min_u32 v160, v160, v143\n\t" \
    "v_max3_u32 v161, v129, v131, v133\n\t" \
    "v_max3_u32 v161, v161, v135, v137\n\t" \
    "v_max3_u32 v161, v161, v139, v141\n\t" \
    "v_max_u32 v161, v161, v143\n\t" \
    "v_cmp_eq_u32 vcc, %4, v160\n\t" \
    "v_cmp_eq_u32 s[64:65], %4, v161\n\t" \
    "s_nop 0\n\t" \
    "s_and_b64 vcc, vcc, s[64:65]\n\t" \
    "s_cmp_eq_u64 vcc, -1\n\t" \
    "s_cbranch_scc1 2f\n\t" \
    "buffer_load_dwordx4 v[128:131], %1, %2, %3 offen offset:0 sc1\n\t" \
    "buffer_load_dwordx4 v[132:135], %1, %2, %3 offen offset:1024 sc1\n\t" \
    "buffer_load_dwordx4 v[136:139], %1, %2, %3 offen offset:2048 sc1\n\t" \
    "buffer_load_dwordx4 v[140:143], %1, %2, %3 offen offset:3072 sc1\n\t" \
    "s_add_u32 s66, s66, 1\n\t" \
    "s_cmp_lt_u32 s66, 0x400000\n\t" \
    "s_cbranch_scc1 1b\n\t" \
    "s_waitcnt vmcnt(0)\n\t" \
    "2:\n\t"
#define ASM_TAGI_B \
    "buffer_load_dwordx4 v[144:147], %1, %2, %3 offen offset:0 sc1\n\t" \
    "buffer_load_dwordx4 v[148:151], %1, %2, %3 offen offset:1024 sc1\n\t" \
    "buffer_load_dwordx4 v[152:155], %1, %2, %3 offen offset:2048 sc1\n\t" \
    "buffer_load_dwordx4 v[156:159], %1, %2, %3 offen offset:3072 sc1\n\t"
#define ASM_TAGTRY_B \
    "s_waitcnt vmcnt(0)\n\t" \
    "v_min3_u32 v160, v145, v147, v149\n\t" \
    "v_min3_u32 v160, v160, v151, v153\n\t" \
    "v_min3_u32 v160, v160, v155, v157\n\t" \
    "v_min_u32 v160, v160, v159\n\t" \
    "v_max3_u32 v161, v145, v147, v149\n\t" \
    "v_max3_u32 v161, v161, v151, v153\n\t" \
    "v_max3_u32 v161, v161, v155, v157\n\t" \
    "v_max_u32 v161, v161, v159\n\t" \
    "v_cmp_eq_u32 vcc, %4, v160\n\t" \
    "v_cmp_eq_u32 s[64:65], %4, v161\n\t" \
    "s_nop 0\n\t" \
    "s_and_b64 vcc, vcc, s[64:65]\n\t" \
    "s_cmp_eq_u64 vcc, -1\n\t" \
    "s_cbranch_scc1 1f\n\t" \
    "buffer_load_dwordx4 v[144:147], %1, %2, %3 offen offset:0 sc1\n\t" \
    "buffer_load_dwordx4 v[148:151], %1, %2, %3 offen offset:1024 sc1\n\t" \
    "buffer_load_dwordx4 v[152:155], %1, %2, %3 offen offset:2048 sc1\n\t" \
    "buffer_load_dwordx4 v[156:159], %1, %2, %3 offen offset:3072 sc1\n\t" \
    "1:\n\t"
#define ASM_TAGSPIN_B \
    "s_mov_b32 s66, 0\n\t" \
    "1:\n\t" \
    "s_waitcnt vmcnt(0)\n\t" \
    "v_min3_u32 v160, v145, v147, v149\n\t" \
    "v_min3_u32 v160, v160, v151, v153\n\t" \
    "v_min3_u32 v160, v160, v155, v157\n\t" \
    "v_min_u32 v160, v160, v159\n\t" \
    "v_max3_u32 v161, v145, v147, v149\n\t" \
    "v_max3_u32 v161, v161, v151, v153\n\t" \
    "v_max3_u32 v161, v161, v155, v157\n\t" \
    "v_max_u32 v161, v161, v159\n\t" \
    "v_cmp_eq_u32 vcc, %4, v160\n\t" \
    "v_cmp_eq_u32 s[64:65], %4, v161\n\t" \
    "s_nop 0\n\t" \
    "s_and_b64 vcc, vcc, s[64:65]\n\t" \
    "s_cmp_eq_u64 vcc, -1\n\t" \
    "s_cbranch_scc1 2f\n\t" \
    "buffer_load_dwordx4 v[144:147], %1, %2, %3 offen offset:0 sc1\n\t" \
    "buffer_load_dwordx4 v[148:151], %1, %2, %3 offen offset:1024 sc1\n\t" \
    "buffer_load_dwordx4 v[152:155], %1, %2, %3 offen offset:2048 sc1\n\t" \
    "buffer_load_dwordx4 v[156:159], %1, %2, %3 offen offset:3072 sc1\n\t" \
    "s_add_u32 s66, s66, 1\n\t" \
    "s_cmp_lt_u32 s66, 0x400000\n\t" \
    "s_cbranch_scc1 1b\n\t" \
    "s_waitcnt vmcnt(0)\n\t" \
    "2:\n\t"
#define ASM_KV \
    "s_mov_b32 %2, 0\n\t" \
    "5:\n\t" \
    "v_mov_b32 v196, %5\n\t" \
    "ds_read_b32 v196, v196\n\t" \
    "s_waitcnt lgkmcnt(0)\n\t" \
    "v_readfirstlane_b32 vcc_lo, v196\n\t" \
    "s_add_u32 %2, %2, 1\n\t" \
    "s_cmp_ge_u32 vcc_lo, 3\n\t" \
    "s_cbranch_scc1 50f\n\t" \
    "s_cmp_lt_u32 %2, 0x400000\n\t" \
    "s_cbranch_scc1 5b\n\t" \
    "50:\n\t" \
    "ds_read_b128 v[196:199], %4 offset:0\n\t" \
    "ds_read_b64_tr_b16 v[200:201], %3 offset:0\n\t" \
    "ds_read_b64_tr_b16 v[202:203], %3 offset:128\n\t" \
    "ds_read_b64_tr_b16 v[204:205], %3 offset:16384\n\t" \
    "ds_read_b64_tr_b16 v[206:207], %3 offset:16512\n\t" \
    "ds_read_b128 v[208:211], %4 offset:64\n\t" \
    "ds_read_b64_tr_b16 v[212:213], %3 offset:1024\n\t" \
    "ds_read_b64_tr_b16 v[214:215], %3 offset:1152\n\t" \
    "ds_read_b64_tr_b16 v[216:217], %3 offset:17408\n\t" \
    "ds_read_b64_tr_b16 v[218:219], %3 offset:17536\n\t" \
    "ds_read_b128 v[220:223], %4 offset:128\n\t" \
    "ds_read_b64_tr_b16 v[224:225], %3 offset:2048\n\t" \
    "ds_read_b64_tr_b16 v[226:227], %3 offset:2176\n\t" \
    "ds_read_b64_tr_b16 v[228:229], %3 offset:18432\n\t" \
    "ds_read_b64_tr_b16 v[230:231], %3 offset:18560\n\t" \
    "s_waitcnt lgkmcnt(10)\n\t" \
    "v_mfma_f32_16x16x32_bf16 %0, v[200:203], v[196:199], 0\n\t" \
    "v_mfma_f32_16x16x32_bf16 %1, v[196:199], v[204:207], 0\n\t" \
    "ds_read_b128 v[196:199], %4 offset:192\n\t" \
    "ds_read_b64_tr_b16 v[200:201], %3 offset:3072\n\t" \
    "ds_read_b64_tr_b16 v[202:203], %3 offset:3200\n\t" \
    "ds_read_b64_tr_b16 v[204:205], %3 offset:19456\n\t" \
    "ds_read_b64_tr_b16 v[206:207], %3 offset:19584\n\t" \
    "s_waitcnt lgkmcnt(10)\n\t" \
    "v_mfma_f32_16x16x32_bf16 %0, v[212:215], v[208:211], %0\n\t" \
    "v_mfma_f32_16x16x32_bf16 %1, v[208:211], v[216:219], %1\n\t" \
    "ds_read_b128 v[208:211], %4 offset:256\n\t" \
    "ds_read_b64_tr_b16 v[212:213], %3 offset:4096\n\t" \
    "ds_read_b64_tr_b16 v[214:215], %3 offset:4224\n\t" \
    "ds_read_b64_tr_b16 v[216:217], %3 offset:20480\n\t" \
    "ds_read_b64_tr_b16 v[218:219], %3 offset:20608\n\t" \
    "s_waitcnt lgkmcnt(10)\n\t" \
    "v_mfma_f32_16x16x32_bf16 %0, v[224:227], v[220:223], %0\n\t" \
    "v_mfma_f32_16x16x32_bf16 %1, v[220:223], v[228:231], %1\n\t" \
    "ds_read_b128 v[220:223], %4 offset:320\n\t" \
    "ds_read_b64_tr_b16 v[224:225], %3 offset:5120\n\t" \
    "ds_read_b64_tr_b16 v[226:227], %3 offset:5248\n\t" \
    "ds_read_b64_tr_b16 v[228:229], %3 offset:21504\n\t" \
    "ds_read_b64_tr_b16 v[230:231], %3 offset:21632\n\t" \
    "s_waitcnt lgkmcnt(10)\n\t" \
    "v_mfma_f32_16x16x32_bf16 %0, v[200:203], v[196:199], %0\n\t" \
    "v_mfma_f32_16x16x32_bf16 %1, v[196:199], v[204:207], %1\n\t" \
    "ds_read_b128 v[196:199], %4 offset:384\n\t" \
    "ds_read_b64_tr_b16 v[200:201], %3 offset:6144\n\t" \
    "ds_read_b64_tr_b16 v[202:203], %3 offset:6272\n\t" \
    "ds_read_b64_tr_b16 v[204:205], %3 offset:22528\n\t" \
    "ds_read_b64_tr_b16 v[206:207], %3 offset:22656\n\t" \
    "s_waitcnt lgkmcnt(10)\n\t" \
    "v_mfma_f32_16x16x32_bf16 %0, v[212:215], v[208:211], %0\n\t" \
    "v_mfma_f32_16x16x32_bf16 %1, v[208:211], v[216:219], %1\n\t" \
    "ds_read_b128 v[208:211], %4 offset:448\n\t" \
    "ds_read_b64_tr_b16 v[212:213], %3 offset:7168\n\t" \
    "ds_read_b64_tr_b16 v[214:215], %3 offset:7296\n\t" \
    "ds_read_b64_tr_b16 v[216:217], %3 offset:23552\n\t" \
    "ds_read_b64_tr_b16 v[218:219], %3 offset:23680\n\t" \
    "s_waitcnt lgkmcnt(10)\n\t" \
    "v_mfma_f32_16x16x32_bf16 %0, v[224:227], v[220:223], %0\n\t" \
    "v_mfma_f32_16x16x32_bf16 %1, v[220:223], v[228:231], %1\n\t" \
    "ds_read_b128 v[220:223], %4 offset:512\n\t" \
    "ds_read_b64_tr_b16 v[224:225], %3 offset:8192\n\t" \
    "ds_read_b64_tr_b16 v[226:227], %3 offset:8320\n\t" \
    "ds_read_b64_tr_b16 v[228:229], %3 offset:24576\n\t" \
    "ds_read_b64_tr_b16 v[230:231], %3 offset:24704\n\t" \
    "s_waitcnt lgkmcnt(10)\n\t" \
    "v_mfma_f32_16x16x32_bf16 %0, v[200:203], v[196:199], %0\n\t" \
    "v_mfma_f32_16x16x32_bf16 %1, v[196:199], v[204:207], %1\n\t" \
    "ds_read_b128 v[196:199], %4 offset:576\n\t" \
    "ds_read_b64_tr_b16 v[200:201], %3 offset:9216\n\t" \
    "ds_read_b64_tr_b16 v[202:203], %3 offset:9344\n\t" \
    "ds_read_b64_tr_b16 v[204:205], %3 offset:25600\n\t" \
    "ds_read_b64_tr_b16 v[206:207], %3 offset:25728\n\t" \
    "s_waitcnt lgkmcnt(10)\n\t" \
    "v_mfma_f32_16x16x32_bf16 %0, v[212:215], v[208:211], %0\n\t" \
    "v_mfma_f32_16x16x32_bf16 %1, v[208:211], v[216:219], %1\n\t" \
    "ds_read_b128 v[208:211], %4 offset:640\n\t" \
    "ds_read_b64_tr_b16 v[212:213], %3 offset:10240\n\t" \
    "ds_read_b64_tr_b16 v[214:215], %3 offset:10368\n\t" \
    "ds_read_b64_tr_b16 v[216:217], %3 offset:26624\n\t" \
    "ds_read_b64_tr_b16 v[218:219], %3 offset:26752\n\t" \
    "s_waitcnt lgkmcnt(10)\n\t" \
    "v_mfma_f32_16x16x32_bf16 %0, v[224:227], v[220:223], %0\n\t" \
    "v_mfma_f32_16x16x32_bf16 %1, v[220:223], v[228:231], %1\n\t" \
    "ds_read_b128 v[220:223], %4 offset:704\n\t" \
    "ds_read_b64_tr_b16 v[224:225], %3 offset:11264\n\t" \
    "ds_read_b64_tr_b16 v[226:227], %3 offset:11392\n\t" \
    "ds_read_b64_tr_b16 v[228:229], %3 offset:27648\n\t" \
    "ds_read_b64_tr_b16 v[230:231], %3 offset:27776\n\t" \
    "s_waitcnt lgkmcnt(10)\n\t" \
    "v_mfma_f32_16x16x32_bf16 %0, v[200:203], v[196:199], %0\n\t" \
    "v_mfma_f32_16x16x32_bf16 %1, v[196:199], v[204:207], %1\n\t" \
    "ds_read_b128 v[196:199], %4 offset:768\n\t" \
    "ds_read_b64_tr_b16 v[200:201], %3 offset:12288\n\t" \
    "ds_read_b64_tr_b16 v[202:203], %3 offset:12416\n\t" \
    "ds_read_b64_tr_b16 v[204:205], %3 offset:28672\n\t" \
    "ds_read_b64_tr_b16 v[206:207], %3 offset:28800\n\t" \
    "s_waitcnt lgkmcnt(10)\n\t" \
    "v_mfma_f32_16x16x32_bf16 %0, v[212:215], v[208:211], %0\n\t" \
    "v_mfma_f32_16x16x32_bf16 %1, v[208:211], v[216:219], %1\n\t" \
    "ds_read_b128 v[208:211], %4 offset:832\n\t" \
    "ds_read_b64_tr_b16 v[212:213], %3 offset:13312\n\t" \
    "ds_read_b64_tr_b16 v[214:215], %3 offset:13440\n\t" \
    "ds_read_b64_tr_b16 v[216:217], %3 offset:29696\n\t" \
    "ds_read_b64_tr_b16 v[218:219], %3 offset:29824\n\t" \
    "s_waitcnt lgkmcnt(10)\n\t" \
    "v_mfma_f32_16x16x32_bf16 %0, v[224:227], v[220:223], %0\n\t" \
    "v_mfma_f32_16x16x32_bf16 %1, v[220:223], v[228:231], %1\n\t" \
    "ds_read_b128 v[220:223], %4 offset:896\n\t" \
    "ds_read_b64_tr_b16 v[224:225], %3 offset:14336\n\t" \
    "ds_read_b64_tr_b16 v[226:227], %3 offset:14464\n\t" \
    "ds_read_b64_tr_b16 v[228:229], %3 offset:30720\n\t" \
    "ds_read_b64_tr_b16 v[230:231], %3 offset:30848\n\t" \
    "s_waitcnt lgkmcnt(10)\n\t" \
    "v_mfma_f32_16x16x32_bf16 %0, v[200:203], v[196:199], %0\n\t" \
    "v_mfma_f32_16x16x32_bf16 %1, v[196:199], v[204:207], %1\n\t" \
    "ds_read_b128 v[196:199], %4 offset:960\n\t" \
    "ds_read_b64_tr_b16 v[200:201], %3 offset:15360\n\t" \
    "ds_read_b64_tr_b16 v[202:203], %3 offset:15488\n\t" \
    "ds_read_b64_tr_b16 v[204:205], %3 offset:31744\n\t" \
    "ds_read_b64_tr_b16 v[206:207], %3 offset:31872\n\t" \
    "s_waitcnt lgkmcnt(10)\n\t" \
    "v_mfma_f32_16x16x32_bf16 %0, v[212:215], v[208:211], %0\n\t" \
    "v_mfma_f32_16x16x32_bf16 %1, v[208:211], v[216:219], %1\n\t" \
    "s_waitcnt lgkmcnt(5)\n\t" \
    "v_mfma_f32_16x16x32_bf16 %0, v[224:227], v[220:223], %0\n\t" \
    "v_mfma_f32_16x16x32_bf16 %1, v[220:223], v[228:231], %1\n\t" \
    "s_waitcnt lgkmcnt(0)\n\t" \
    "v_mfma_f32_16x16x32_bf16 %0, v[200:203], v[196:199], %0\n\t" \
    "v_mfma_f32_16x16x32_bf16 %1, v[196:199], v[204:207], %1\n\t" \
    "s_nop 7\n\t"
#define CLOB_KV "v196","v197","v198","v199","v200","v201","v202","v203","v204","v205","v206","v207","v208","v209","v210","v211","v212","v213","v214","v215","v216","v217","v218","v219","v220","v221","v222","v223","v224","v225","v226","v227","v228","v229","v230","v231"
#define ASM_UP \
    "s_cmp_eq_u32 %21, 3\n\t" \
    "s_cbranch_scc1 91f\n\t" \
    "v_add_u32 v213, 27648, %8\n\t" \
    "v_add_u32 v214, 55296, %7\n\t" \
    "v_add_u32 v215, 82944, %8\n\t" \
    "s_add_u32 %4, %6, 0\n\t" \
    "s_and_b32 %4, %4, 31\n\t" \
    "s_lshl_b32 %4, %4, 5\n\t" \
    "v_add_u32 v192, %4, %12\n\t" \
    "s_add_u32 %4, %6, 4\n\t" \
    "s_and_b32 %4, %4, 31\n\t" \
    "s_lshl_b32 %4, %4, 5\n\t" \
    "v_add_u32 v193, %4, %12\n\t" \
    "s_add_u32 %4, %6, 8\n\t" \
    "s_and_b32 %4, %4, 31\n\t" \
    "s_lshl_b32 %4, %4, 5\n\t" \
    "v_add_u32 v194, %4, %12\n\t" \
    "s_add_u32 %4, %6, 12\n\t" \
    "s_and_b32 %4, %4, 31\n\t" \
    "s_lshl_b32 %4, %4, 5\n\t" \
    "v_add_u32 v195, %4, %12\n\t" \
    "s_add_u32 %4, %6, 16\n\t" \
    "s_and_b32 %4, %4, 31\n\t" \
    "s_lshl_b32 %4, %4, 5\n\t" \
    "v_add_u32 v196, %4, %12\n\t" \
    "s_add_u32 %4, %6, 20\n\t" \
    "s_and_b32 %4, %4, 31\n\t" \
    "s_lshl_b32 %4, %4, 5\n\t" \
    "v_add_u32 v197, %4, %12\n\t" \
    "s_add_u32 %4, %6, 24\n\t" \
    "s_and_b32 %4, %4, 31\n\t" \
    "s_lshl_b32 %4, %4, 5\n\t" \
    "v_add_u32 v198, %4, %12\n\t" \
    "s_add_u32 %4, %6, 28\n\t" \
    "s_and_b32 %4, %4, 31\n\t" \
    "s_lshl_b32 %4, %4, 5\n\t" \
    "v_add_u32 v199, %4, %12\n\t" \
    "v_xor_b32 v208, 0, %11\n\t" \
    "v_add_u32 v200, %9, v208\n\t" \
    "v_add_u32 v204, %10, v208\n\t" \
    "v_xor_b32 v208, 32, %11\n\t" \
    "v_add_u32 v201, %9, v208\n\t" \
    "v_add_u32 v205, %10, v208\n\t" \
    "v_xor_b32 v208, 64, %11\n\t" \
    "v_add_u32 v202, %9, v208\n\t" \
    "v_add_u32 v206, %10, v208\n\t" \
    "v_xor_b32 v208, 96, %11\n\t" \
    "v_add_u32 v203, %9, v208\n\t" \
    "v_add_u32 v207, %10, v208\n\t" \
    "s_mov_b32 %4, 0\n\t" \
    "7:\n\t" \
    "v_mov_b32 v208, %13\n\t" \
    "ds_read_b32 v208, v208\n\t" \
    "s_waitcnt lgkmcnt(0)\n\t" \
    "v_readfirstlane_b32 vcc_lo, v208\n\t" \
    "s_add_u32 %4, %4, 1\n\t" \
    "s_cmp_ge_u32 vcc_lo, 7\n\t" \
    "s_cbranch_scc1 70f\n\t" \
    "s_sleep 1\n\t" \
    "s_cmp_lt_u32 %4, 0x400000\n\t" \
    "s_cbranch_scc1 7b\n\t" \
    "70:\n\t" \
    "s_setprio 2\n\t" \
    "s_waitcnt vmcnt(4)\n\t" \
    "ds_read_b64_tr_b16 v[64:65], v200 offset:0\n\t" \
    "ds_read_b64_tr_b16 v[66:67], v200 offset:512\n\t" \
    "ds_read_b64_tr_b16 v[68:69], v201 offset:0\n\t" \
    "ds_read_b64_tr_b16 v[70:71], v201 offset:512\n\t" \
    "ds_read_b64_tr_b16 v[72:73], v202 offset:0\n\t" \
    "ds_read_b64_tr_b16 v[74:75], v202 offset:512\n\t" \
    "ds_read_b64_tr_b16 v[76:77], v203 offset:0\n\t" \
    "ds_read_b64_tr_b16 v[78:79], v203 offset:512\n\t" \
    "ds_read_b128 v[96:99], v192 offset:0\n\t" \
    "ds_read_b128 v[100:103], v192 offset:16640\n\t" \
    "ds_read_b128 v[104:107], v192 offset:33280\n\t" \
    "ds_read_b128 v[108:111], v192 offset:49920\n\t" \
    "s_waitcnt vmcnt(0) lgkmcnt(0)\n\t" \
    "v_mfma_f32_16x16x32_bf16 v[128:131], v[64:67], v[96:99], 0\n\t" \
    "ds_read_b64_tr_b16 v[80:81], v200 offset:4096\n\t" \
    "v_mfma_f32_16x16x32_bf16 v[132:135], v[64:67], v[100:103], 0\n\t" \
    "ds_read_b64_tr_b16 v[82:83], v200 offset:4608\n\t" \
    "s_add_u32 %4, %6, 4\n\t" \
    "s_and_b32 %4, %4, 31\n\t" \
    "s_add_u32 m0, %9, 0\n\t" \
    "s_mul_i32 %3, %4, 57344\n\t" \
    "buffer_load_dwordx4 %7, %5, %3 offen offset:0 lds\n\t" \
    "v_mfma_f32_16x16x32_bf16 v[136:139], v[64:67], v[104:107], 0\n\t" \
    "ds_read_b64_tr_b16 v[84:85], v201 offset:4096\n\t" \
    "v_mfma_f32_16x16x32_bf16 v[140:143], v[64:67], v[108:111], 0\n\t" \
    "ds_read_b64_tr_b16 v[86:87], v201 offset:4608\n\t" \
    "buffer_load_dwordx4 v213, %5, %3 offen offset:1024 lds\n\t" \
    "v_mfma_f32_16x16x32_bf16 v[156:159], v[68:71], v[108:111], 0\n\t" \
    "ds_read_b64_tr_b16 v[88:89], v202 offset:4096\n\t" \
    "v_mfma_f32_16x16x32_bf16 v[152:155], v[68:71], v[104:107], 0\n\t" \
    "ds_read_b64_tr_b16 v[90:91], v202 offset:4608\n\t" \
    "buffer_load_dwordx4 v214, %5, %3 offen offset:2048 lds\n\t" \
    "v_mfma_f32_16x16x32_bf16 v[148:151], v[68:71], v[100:103], 0\n\t" \
    "ds_read_b64_tr_b16 v[92:93], v203 offset:4096\n\t" \
    "v_mfma_f32_16x16x32_bf16 v[144:147], v[68:71], v[96:99], 0\n\t" \
    "ds_read_b64_tr_b16 v[94:95], v203 offset:4608\n\t" \
    "buffer_load_dwordx4 v215, %5, %3 offen offset:3072 lds\n\t" \
    "v_mfma_f32_16x16x32_bf16 v[160:163], v[72:75], v[96:99], 0\n\t" \
    "ds_read_b128 v[112:115], v192 offset:64\n\t" \
    "v_mfma_f32_16x16x32_bf16 v[164:167], v[72:75], v[100:103], 0\n\t" \
    "ds_read_b128 v[116:119], v192 offset:16704\n\t" \
    "v_mfma_f32_16x16x32_bf16 v[168:171], v[72:75], v[104:107], 0\n\t" \
    "ds_read_b128 v[120:123], v192 offset:33344\n\t" \
    "v_mfma_f32_16x16x32_bf16 v[172:175], v[72:75], v[108:111], 0\n\t" \
    "ds_read_b128 v[124:127], v192 offset:49984\n\t" \
    "v_mfma_f32_16x16x32_bf16 v[188:191], v[76:79], v[108:111], 0\n\t" \
    "v_mfma_f32_16x16x32_bf16 v[184:187], v[76:79], v[104:107], 0\n\t" \
    "v_mfma_f32_16x16x32_bf16 v[180:183], v[76:79], v[100:103], 0\n\t" \
    "v_mfma_f32_16x16x32_bf16 v[176:179], v[76:79], v[96:99], 0\n\t" \
    "s_waitcnt vmcnt(0) lgkmcnt(0)\n\t" \
    "v_mfma_f32_16x16x32_bf16 v[128:131], v[80:83], v[112:115], v[128:131]\n\t" \
    "ds_read_b64_tr_b16 v[64:65], v200 offset:0\n\t" \
    "v_mfma_f32_16x16x32_bf16 v[132:135], v[80:83], v[116:119], v[132:135]\n\t" \
    "ds_read_b64_tr_b16 v[66:67], v200 offset:512\n\t" \
    "s_add_u32 %4, %6, 6\n\t" \
    "s_and_b32 %4, %4, 31\n\t" \
    "s_add_u32 m0, %9, 4096\n\t" \
    "s_mul_i32 %3, %4, 57344\n\t" \
    "buffer_load_dwordx4 %7, %5, %3 offen offset:0 lds\n\t" \
    "v_mfma_f32_16x16x32_bf16 v[136:139], v[80:83], v[120:123], v[136:139]\n\t" \
    "ds_read_b64_tr_b16 v[68:69], v201 offset:0\n\t" \
    "v_mfma_f32_16x16x32_bf16 v[140:143], v[80:83], v[124:127], v[140:143]\n\t" \
    "ds_read_b64_tr_b16 v[70:71], v201 offset:512\n\t" \
    "buffer_load_dwordx4 v213, %5, %3 offen offset:1024 lds\n\t" \
    "v_mfma_f32_16x16x32_bf16 v[156:159], v[84:87], v[124:127], v[156:159]\n\t" \
    "ds_read_b64_tr_b16 v[72:73], v202 offset:0\n\t" \
    "v_mfma_f32_16x16x32_bf16 v[152:155], v[84:87], v[120:123], v[152:155]\n\t" \
    "ds_read_b64_tr_b16 v[74:75], v202 offset:512\n\t" \
    "buffer_load_dwordx4 v214, %5, %3 offen offset:2048 lds\n\t" \
    "v_mfma_f32_16x16x32_bf16 v[148:151], v[84:87], v[116:119], v[148:151]\n\t" \
    "ds_read_b64_tr_b16 v[76:77], v203 offset:0\n\t" \
    "v_mfma_f32_16x16x32_bf16 v[144:147], v[84:87], v[112:115], v[144:147]\n\t" \
    "ds_read_b64_tr_b16 v[78:79], v203 offset:512\n\t" \
    "buffer_load_dwordx4 v215, %5, %3 offen offset:3072 lds\n\t" \
    "v_mfma_f32_16x16x32_bf16 v[160:163], v[88:91], v[112:115], v[160:163]\n\t" \
    "ds_read_b128 v[96:99], v193 offset:0\n\t" \
    "v_mfma_f32_16x16x32_bf16 v[164:167], v[88:91], v[116:119], v[164:167]\n\t" \
    "ds_read_b128 v[100:103], v193 offset:16640\n\t" \
    "v_mfma_f32_16x16x32_bf16 v[168:171], v[88:91], v[120:123], v[168:171]\n\t" \
    "ds_read_b128 v[104:107], v193 offset:33280\n\t" \
    "v_mfma_f32_16x16x32_bf16 v[172:175], v[88:91], v[124:127], v[172:175]\n\t" \
    "ds_read_b128 v[108:111], v193 offset:49920\n\t" \
    "v_mfma_f32_16x16x32_bf16 v[188:191], v[92:95], v[124:127], v[188:191]\n\t" \
    "v_mfma_f32_16x16x32_bf16 v[184:187], v[92:95], v[120:123], v[184:187]\n\t" \
    "v_mfma_f32_16x16x32_bf16 v[180:183], v[92:95], v[116:119], v[180:183]\n\t" \
    "v_mfma_f32_16x16x32_bf16 v[176:179], v[92:95], v[112:115], v[176:179]\n\t" \
    "s_waitcnt vmcnt(0) lgkmcnt(0)\n\t" \
    "v_mfma_f32_16x16x32_bf16 v[128:131], v[64:67], v[96:99], v[128:131]\n\t" \
    "ds_read_b64_tr_b16 v[80:81], v200 offset:4096\n\t" \
    "v_mfma_f32_16x16x32_bf16 v[132:135], v[64:67], v[100:103], v[132:135]\n\t" \
    "ds_read_b64_tr_b16 v[82:83], v200 offset:4608\n\t" \
    "s_add_u32 %4, %6, 8\n\t" \
    "s_and_b32 %4, %4, 31\n\t" \
    "s_add_u32 m0, %9, 0\n\t" \
    "s_mul_i32 %3, %4, 57344\n\t" \
    "buffer_load_dwordx4 %7, %5, %3 offen offset:0 lds\n\t" \
    "v_mfma_f32_16x16x32_bf16 v[136:139], v[64:67], v[104:107], v[136:139]\n\t" \
    "ds_read_b64_tr_b16 v[84:85], v201 offset:4096\n\t" \
    "v_mfma_f32_16x16x32_bf16 v[140:143], v[64:67], v[108:111], v[140:143]\n\t" \
    "ds_read_b64_tr_b16 v[86:87], v201 offset:4608\n\t" \
    "buffer_load_dwordx4 v213, %5, %3 offen offset:1024 lds\n\t" \
    "v_mfma_f32_16x16x32_bf16 v[156:159], v[68:71], v[108:111], v[156:159]\n\t" \
    "ds_read_b64_tr_b16 v[88:89], v202 offset:4096\n\t" \
    "v_mfma_f32_16x16x32_bf16 v[152:155], v[68:71], v[104:107], v[152:155]\n\t" \
    "ds_read_b64_tr_b16 v[90:91], v202 offset:4608\n\t" \
    "buffer_load_dwordx4 v214, %5, %3 offen offset:2048 lds\n\t" \
    "v_mfma_f32_16x16x32_bf16 v[148:151], v[68:71], v[100:103], v[148:151]\n\t" \
    "ds_read_b64_tr_b16 v[92:93], v203 offset:4096\n\t" \
    "v_mfma_f32_16x16x32_bf16 v[144:147], v[68:71], v[96:99], v[144:147]\n\t" \
    "ds_read_b64_tr_b16 v[94:95], v203 offset:4608\n\t" \
    "buffer_load_dwordx4 v215, %5, %3 offen offset:3072 lds\n\t" \
    "v_mfma_f32_16x16x32_bf16 v[160:163], v[72:75], v[96:99], v[160:163]\n\t" \
    "ds_read_b128 v[112:115], v193 offset:64\n\t" \
    "v_mfma_f32_16x16x32_bf16 v[164:167], v[72:75], v[100:103], v[164:167]\n\t" \
    "ds_read_b128 v[116:119], v193 offset:16704\n\t" \
    "v_mfma_f32_16x16x32_bf16 v[168:171], v[72:75], v[104:107], v[168:171]\n\t" \
    "ds_read_b128 v[120:123], v193 offset:33344\n\t" \
    "v_mfma_f32_16x16x32_bf16 v[172:175], v[72:75], v[108:111], v[172:175]\n\t" \
    "ds_read_b128 v[124:127], v193 offset:49984\n\t" \
    "v_mfma_f32_16x16x32_bf16 v[188:191], v[76:79], v[108:111], v[188:191]\n\t" \
    "v_mfma_f32_16x16x32_bf16 v[184:187], v[76:79], v[104:107], v[184:187]\n\t" \
    "v_mfma_f32_16x16x32_bf16 v[180:183], v[76:79], v[100:103], v[180:183]\n\t" \
    "v_mfma_f32_16x16x32_bf16 v[176:179], v[76:79], v[96:99], v[176:179]\n\t" \
    "s_waitcnt vmcnt(0) lgkmcnt(0)\n\t" \
    "v_mfma_f32_16x16x32_bf16 v[128:131], v[80:83], v[112:115], v[128:131]\n\t" \
    "ds_read_b64_tr_b16 v[64:65], v200 offset:0\n\t" \
    "v_mfma_f32_16x16x32_bf16 v[132:135], v[80:83], v[116:119], v[132:135]\n\t" \
    "ds_read_b64_tr_b16 v[66:67], v200 offset:512\n\t" \
    "s_add_u32 %4, %6, 10\n\t" \
    "s_and_b32 %4, %4, 31\n\t" \
    "s_add_u32 m0, %9, 4096\n\t" \
    "s_mul_i32 %3, %4, 57344\n\t" \
    "buffer_load_dwordx4 %7, %5, %3 offen offset:0 lds\n\t" \
    "v_mfma_f32_16x16x32_bf16 v[136:139], v[80:83], v[120:123], v[136:139]\n\t" \
    "ds_read_b64_tr_b16 v[68:69], v201 offset:0\n\t" \
    "v_mfma_f32_16x16x32_bf16 v[140:143], v[80:83], v[124:127], v[140:143]\n\t" \
    "ds_read_b64_tr_b16 v[70:71], v201 offset:512\n\t" \
    "buffer_load_dwordx4 v213, %5, %3 offen offset:1024 lds\n\t" \
    "v_mfma_f32_16x16x32_bf16 v[156:159], v[84:87], v[124:127], v[156:159]\n\t" \
    "ds_read_b64_tr_b16 v[72:73], v202 offset:0\n\t" \
    "v_mfma_f32_16x16x32_bf16 v[152:155], v[84:87], v[120:123], v[152:155]\n\t" \
    "ds_read_b64_tr_b16 v[74:75], v202 offset:512\n\t" \
    "buffer_load_dwordx4 v214, %5, %3 offen offset:2048 lds\n\t" \
    "v_mfma_f32_16x16x32_bf16 v[148:151], v[84:87], v[116:119], v[148:151]\n\t" \
    "ds_read_b64_tr_b16 v[76:77], v203 offset:0\n\t" \
    "v_mfma_f32_16x16x32_bf16 v[144:147], v[84:87], v[112:115], v[144:147]\n\t" \
    "ds_read_b64_tr_b16 v[78:79], v203 offset:512\n\t" \
    "buffer_load_dwordx4 v215, %5, %3 offen offset:3072 lds\n\t" \
    "v_mfma_f32_16x16x32_bf16 v[160:163], v[88:91], v[112:115], v[160:163]\n\t" \
    "ds_read_b128 v[96:99], v194 offset:0\n\t" \
    "v_mfma_f32_16x16x32_bf16 v[164:167], v[88:91], v[116:119], v[164:167]\n\t" \
    "ds_read_b128 v[100:103], v194 offset:16640\n\t" \
    "v_mfma_f32_16x16x32_bf16 v[168:171], v[88:91], v[120:123], v[168:171]\n\t" \
    "ds_read_b128 v[104:107], v194 offset:33280\n\t" \
    "v_mfma_f32_16x16x32_bf16 v[172:175], v[88:91], v[124:127], v[172:175]\n\t" \
    "ds_read_b128 v[108:111], v194 offset:49920\n\t" \
    "v_mfma_f32_16x16x32_bf16 v[188:191], v[92:95], v[124:127], v[188:191]\n\t" \
    "v_mfma_f32_16x16x32_bf16 v[184:187], v[92:95], v[120:123], v[184:187]\n\t" \
    "v_mfma_f32_16x16x32_bf16 v[180:183], v[92:95], v[116:119], v[180:183]\n\t" \
    "v_mfma_f32_16x16x32_bf16 v[176:179], v[92:95], v[112:115], v[176:179]\n\t" \
    "s_waitcnt vmcnt(0) lgkmcnt(0)\n\t" \
    "v_mfma_f32_16x16x32_bf16 v[128:131], v[64:67], v[96:99], v[128:131]\n\t" \
    "ds_read_b64_tr_b16 v[80:81], v200 offset:4096\n\t" \
    "v_mfma_f32_16x16x32_bf16 v[132:135], v[64:67], v[100:103], v[132:135]\n\t" \
    "ds_read_b64_tr_b16 v[82:83], v200 offset:4608\n\t" \
    "s_add_u32 %4, %6, 12\n\t" \
    "s_and_b32 %4, %4, 31\n\t" \
    "s_add_u32 m0, %9, 0\n\t" \
    "s_mul_i32 %3, %4, 57344\n\t" \
    "buffer_load_dwordx4 %7, %5, %3 offen offset:0 lds\n\t" \
    "v_mfma_f32_16x16x32_bf16 v[136:139], v[64:67], v[104:107], v[136:139]\n\t" \
    "ds_read_b64_tr_b16 v[84:85], v201 offset:4096\n\t" \
    "v_mfma_f32_16x16x32_bf16 v[140:143], v[64:67], v[108:111], v[140:143]\n\t" \
    "ds_read_b64_tr_b16 v[86:87], v201 offset:4608\n\t" \
    "buffer_load_dwordx4 v213, %5, %3 offen offset:1024 lds\n\t" \
    "v_mfma_f32_16x16x32_bf16 v[156:159], v[68:71], v[108:111], v[156:159]\n\t" \
    "ds_read_b64_tr_b16 v[88:89], v202 offset:4096\n\t" \
    "v_mfma_f32_16x16x32_bf16 v[152:155], v[68:71], v[104:107], v[152:155]\n\t" \
    "ds_read_b64_tr_b16 v[90:91], v202 offset:4608\n\t" \
    "buffer_load_dwordx4 v214, %5, %3 offen offset:2048 lds\n\t" \
    "v_mfma_f32_16x16x32_bf16 v[148:151], v[68:71], v[100:103], v[148:151]\n\t" \
    "ds_read_b64_tr_b16 v[92:93], v203 offset:4096\n\t" \
    "v_mfma_f32_16x16x32_bf16 v[144:147], v[68:71], v[96:99], v[144:147]\n\t" \
    "ds_read_b64_tr_b16 v[94:95], v203 offset:4608\n\t" \
    "buffer_load_dwordx4 v215, %5, %3 offen offset:3072 lds\n\t" \
    "v_mfma_f32_16x16x32_bf16 v[160:163], v[72:75], v[96:99], v[160:163]\n\t" \
    "ds_read_b128 v[112:115], v194 offset:64\n\t" \
    "v_mfma_f32_16x16x32_bf16 v[164:167], v[72:75], v[100:103], v[164:167]\n\t" \
    "ds_read_b128 v[116:119], v194 offset:16704\n\t" \
    "v_mfma_f32_16x16x32_bf16 v[168:171], v[72:75], v[104:107], v[168:171]\n\t" \
    "ds_read_b128 v[120:123], v194 offset:33344\n\t" \
    "v_mfma_f32_16x16x32_bf16 v[172:175], v[72:75], v[108:111], v[172:175]\n\t" \
    "ds_read_b128 v[124:127], v194 offset:49984\n\t" \
    "v_mfma_f32_16x16x32_bf16 v[188:191], v[76:79], v[108:111], v[188:191]\n\t" \
    "v_mfma_f32_16x16x32_bf16 v[184:187], v[76:79], v[104:107], v[184:187]\n\t" \
    "v_mfma_f32_16x16x32_bf16 v[180:183], v[76:79], v[100:103], v[180:183]\n\t" \
    "v_mfma_f32_16x16x32_bf16 v[176:179], v[76:79], v[96:99], v[176:179]\n\t" \
    "s_waitcnt vmcnt(0) lgkmcnt(0)\n\t" \
    "v_mfma_f32_16x16x32_bf16 v[128:131], v[80:83], v[112:115], v[128:131]\n\t" \
    "ds_read_b64_tr_b16 v[64:65], v200 offset:0\n\t" \
    "v_mfma_f32_16x16x32_bf16 v[132:135], v[80:83], v[116:119], v[132:135]\n\t" \
    "ds_read_b64_tr_b16 v[66:67], v200 offset:512\n\t" \
    "s_add_u32 %4, %6, 14\n\t" \
    "s_and_b32 %4, %4, 31\n\t" \
    "s_add_u32 m0, %9, 4096\n\t" \
    "s_mul_i32 %3, %4, 57344\n\t" \
    "buffer_load_dwordx4 %7, %5, %3 offen offset:0 lds\n\t" \
    "v_mfma_f32_16x16x32_bf16 v[136:139], v[80:83], v[120:123], v[136:139]\n\t" \
    "ds_read_b64_tr_b16 v[68:69], v201 offset:0\n\t" \
    "v_mfma_f32_16x16x32_bf16 v[140:143], v[80:83], v[124:127], v[140:143]\n\t" \
    "ds_read_b64_tr_b16 v[70:71], v201 offset:512\n\t" \
    "buffer_load_dwordx4 v213, %5, %3 offen offset:1024 lds\n\t" \
    "v_mfma_f32_16x16x32_bf16 v[156:159], v[84:87], v[124:127], v[156:159]\n\t" \
    "ds_read_b64_tr_b16 v[72:73], v202 offset:0\n\t" \
    "v_mfma_f32_16x16x32_bf16 v[152:155], v[84:87], v[120:123], v[152:155]\n\t" \
    "ds_read_b64_tr_b16 v[74:75], v202 offset:512\n\t" \
    "buffer_load_dwordx4 v214, %5, %3 offen offset:2048 lds\n\t" \
    "v_mfma_f32_16x16x32_bf16 v[148:151], v[84:87], v[116:119], v[148:151]\n\t" \
    "ds_read_b64_tr_b16 v[76:77], v203 offset:0\n\t" \
    "v_mfma_f32_16x16x32_bf16 v[144:147], v[84:87], v[112:115], v[144:147]\n\t" \
    "ds_read_b64_tr_b16 v[78:79], v203 offset:512\n\t" \
    "buffer_load_dwordx4 v215, %5, %3 offen offset:3072 lds\n\t" \
    "v_mfma_f32_16x16x32_bf16 v[160:163], v[88:91], v[112:115], v[160:163]\n\t" \
    "ds_read_b128 v[96:99], v195 offset:0\n\t" \
    "v_mfma_f32_16x16x32_bf16 v[164:167], v[88:91], v[116:119], v[164:167]\n\t" \
    "ds_read_b128 v[100:103], v195 offset:16640\n\t" \
    "v_mfma_f32_16x16x32_bf16 v[168:171], v[88:91], v[120:123], v[168:171]\n\t" \
    "ds_read_b128 v[104:107], v195 offset:33280\n\t" \
    "v_mfma_f32_16x16x32_bf16 v[172:175], v[88:91], v[124:127], v[172:175]\n\t" \
    "ds_read_b128 v[108:111], v195 offset:49920\n\t" \
    "v_mfma_f32_16x16x32_bf16 v[188:191], v[92:95], v[124:127], v[188:191]\n\t" \
    "v_mfma_f32_16x16x32_bf16 v[184:187], v[92:95], v[120:123], v[184:187]\n\t" \
    "v_mfma_f32_16x16x32_bf16 v[180:183], v[92:95], v[116:119], v[180:183]\n\t" \
    "v_mfma_f32_16x16x32_bf16 v[176:179], v[92:95], v[112:115], v[176:179]\n\t" \
    "s_waitcnt vmcnt(0) lgkmcnt(0)\n\t" \
    "v_mfma_f32_16x16x32_bf16 v[128:131], v[64:67], v[96:99], v[128:131]\n\t" \
    "ds_read_b64_tr_b16 v[80:81], v200 offset:4096\n\t" \
    "v_mfma_f32_16x16x32_bf16 v[132:135], v[64:67], v[100:103], v[132:135]\n\t" \
    "ds_read_b64_tr_b16 v[82:83], v200 offset:4608\n\t" \
    "s_add_u32 %4, %6, 16\n\t" \
    "s_and_b32 %4, %4, 31\n\t" \
    "s_add_u32 m0, %9, 0\n\t" \
    "s_mul_i32 %3, %4, 57344\n\t" \
    "buffer_load_dwordx4 %7, %5, %3 offen offset:0 lds\n\t" \
    "v_mfma_f32_16x16x32_bf16 v[136:139], v[64:67], v[104:107], v[136:139]\n\t" \
    "ds_read_b64_tr_b16 v[84:85], v201 offset:4096\n\t" \
    "v_mfma_f32_16x16x32_bf16 v[140:143], v[64:67], v[108:111], v[140:143]\n\t" \
    "ds_read_b64_tr_b16 v[86:87], v201 offset:4608\n\t" \
    "buffer_load_dwordx4 v213, %5, %3 offen offset:1024 lds\n\t" \
    "v_mfma_f32_16x16x32_bf16 v[156:159], v[68:71], v[108:111], v[156:159]\n\t" \
    "ds_read_b64_tr_b16 v[88:89], v202 offset:4096\n\t" \
    "v_mfma_f32_16x16x32_bf16 v[152:155], v[68:71], v[104:107], v[152:155]\n\t" \
    "ds_read_b64_tr_b16 v[90:91], v202 offset:4608\n\t" \
    "buffer_load_dwordx4 v214, %5, %3 offen offset:2048 lds\n\t" \
    "v_mfma_f32_16x16x32_bf16 v[148:151], v[68:71], v[100:103], v[148:151]\n\t" \
    "ds_read_b64_tr_b16 v[92:93], v203 offset:4096\n\t" \
    "v_mfma_f32_16x16x32_bf16 v[144:147], v[68:71], v[96:99], v[144:147]\n\t" \
    "ds_read_b64_tr_b16 v[94:95], v203 offset:4608\n\t" \
    "buffer_load_dwordx4 v215, %5, %3 offen offset:3072 lds\n\t" \
    "v_mfma_f32_16x16x32_bf16 v[160:163], v[72:75], v[96:99], v[160:163]\n\t" \
    "ds_read_b128 v[112:115], v195 offset:64\n\t" \
    "v_mfma_f32_16x16x32_bf16 v[164:167], v[72:75], v[100:103], v[164:167]\n\t" \
    "ds_read_b128 v[116:119], v195 offset:16704\n\t" \
    "v_mfma_f32_16x16x32_bf16 v[168:171], v[72:75], v[104:107], v[168:171]\n\t" \
    "ds_read_b128 v[120:123], v195 offset:33344\n\t" \
    "v_mfma_f32_16x16x32_bf16 v[172:175], v[72:75], v[108:111], v[172:175]\n\t" \
    "ds_read_b128 v[124:127], v195 offset:49984\n\t" \
    "v_mfma_f32_16x16x32_bf16 v[188:191], v[76:79], v[108:111], v[188:191]\n\t" \
    "v_mfma_f32_16x16x32_bf16 v[184:187], v[76:79], v[104:107], v[184:187]\n\t" \
    "v_mfma_f32_16x16x32_bf16 v[180:183], v[76:79], v[100:103], v[180:183]\n\t" \
    "v_mfma_f32_16x16x32_bf16 v[176:179], v[76:79], v[96:99], v[176:179]\n\t" \
    "s_waitcnt vmcnt(0) lgkmcnt(0)\n\t" \
    "v_mfma_f32_16x16x32_bf16 v[128:131], v[80:83], v[112:115], v[128:131]\n\t" \
    "ds_read_b64_tr_b16 v[64:65], v200 offset:0\n\t" \
    "v_mfma_f32_16x16x32_bf16 v[132:135], v[80:83], v[116:119], v[132:135]\n\t" \
    "ds_read_b64_tr_b16 v[66:67], v200 offset:512\n\t" \
    "s_add_u32 %4, %6, 18\n\t" \
    "s_and_b32 %4, %4, 31\n\t" \
    "s_add_u32 m0, %9, 4096\n\t" \
    "s_mul_i32 %3, %4, 57344\n\t" \
    "buffer_load_dwordx4 %7, %5, %3 offen offset:0 lds\n\t" \
    "v_mfma_f32_16x16x32_bf16 v[136:139], v[80:83], v[120:123], v[136:139]\n\t" \
    "ds_read_b64_tr_b16 v[68:69], v201 offset:0\n\t" \
    "v_mfma_f32_16x16x32_bf16 v[140:143], v[80:83], v[124:127], v[140:143]\n\t" \
    "ds_read_b64_tr_b16 v[70:71], v201 offset:512\n\t" \
    "buffer_load_dwordx4 v213, %5, %3 offen offset:1024 lds\n\t" \
    "v_mfma_f32_16x16x32_bf16 v[156:159], v[84:87], v[124:127], v[156:159]\n\t" \
    "ds_read_b64_tr_b16 v[72:73], v202 offset:0\n\t" \
    "v_mfma_f32_16x16x32_bf16 v[152:155], v[84:87], v[120:123], v[152:155]\n\t" \
    "ds_read_b64_tr_b16 v[74:75], v202 offset:512\n\t" \
    "buffer_load_dwordx4 v214, %5, %3 offen offset:2048 lds\n\t" \
    "v_mfma_f32_16x16x32_bf16 v[148:151], v[84:87], v[116:119], v[148:151]\n\t" \
    "ds_read_b64_tr_b16 v[76:77], v203 offset:0\n\t" \
    "v_mfma_f32_16x16x32_bf16 v[144:147], v[84:87], v[112:115], v[144:147]\n\t" \
    "ds_read_b64_tr_b16 v[78:79], v203 offset:512\n\t" \
    "buffer_load_dwordx4 v215, %5, %3 offen offset:3072 lds\n\t" \
    "v_mfma_f32_16x16x32_bf16 v[160:163], v[88:91], v[112:115], v[160:163]\n\t" \
    "ds_read_b128 v[96:99], v196 offset:0\n\t" \
    "v_mfma_f32_16x16x32_bf16 v[164:167], v[88:91], v[116:119], v[164:167]\n\t" \
    "ds_read_b128 v[100:103], v196 offset:16640\n\t" \
    "v_mfma_f32_16x16x32_bf16 v[168:171], v[88:91], v[120:123], v[168:171]\n\t" \
    "ds_read_b128 v[104:107], v196 offset:33280\n\t" \
    "v_mfma_f32_16x16x32_bf16 v[172:175], v[88:91], v[124:127], v[172:175]\n\t" \
    "ds_read_b128 v[108:111], v196 offset:49920\n\t" \
    "v_mfma_f32_16x16x32_bf16 v[188:191], v[92:95], v[124:127], v[188:191]\n\t" \
    "v_mfma_f32_16x16x32_bf16 v[184:187], v[92:95], v[120:123], v[184:187]\n\t" \
    "v_mfma_f32_16x16x32_bf16 v[180:183], v[92:95], v[116:119], v[180:183]\n\t" \
    "v_mfma_f32_16x16x32_bf16 v[176:179], v[92:95], v[112:115], v[176:179]\n\t" \
    "s_waitcnt vmcnt(0) lgkmcnt(0)\n\t" \
    "v_mfma_f32_16x16x32_bf16 v[128:131], v[64:67], v[96:99], v[128:131]\n\t" \
    "ds_read_b64_tr_b16 v[80:81], v200 offset:4096\n\t" \
    "v_mfma_f32_16x16x32_bf16 v[132:135], v[64:67], v[100:103], v[132:135]\n\t" \
    "ds_read_b64_tr_b16 v[82:83], v200 offset:4608\n\t" \
    "s_add_u32 %4, %6, 20\n\t" \
    "s_and_b32 %4, %4, 31\n\t" \
    "s_add_u32 m0, %9, 0\n\t" \
    "s_mul_i32 %3, %4, 57344\n\t" \
    "buffer_load_dwordx4 %7, %5, %3 offen offset:0 lds\n\t" \
    "v_mfma_f32_16x16x32_bf16 v[136:139], v[64:67], v[104:107], v[136:139]\n\t" \
    "ds_read_b64_tr_b16 v[84:85], v201 offset:4096\n\t" \
    "v_mfma_f32_16x16x32_bf16 v[140:143], v[64:67], v[108:111], v[140:143]\n\t" \
    "ds_read_b64_tr_b16 v[86:87], v201 offset:4608\n\t" \
    "buffer_load_dwordx4 v213, %5, %3 offen offset:1024 lds\n\t" \
    "v_mfma_f32_16x16x32_bf16 v[156:159], v[68:71], v[108:111], v[156:159]\n\t" \
    "ds_read_b64_tr_b16 v[88:89], v202 offset:4096\n\t" \
    "v_mfma_f32_16x16x32_bf16 v[152:155], v[68:71], v[104:107], v[152:155]\n\t" \
    "ds_read_b64_tr_b16 v[90:91], v202 offset:4608\n\t" \
    "buffer_load_dwordx4 v214, %5, %3 offen offset:2048 lds\n\t" \
    "v_mfma_f32_16x16x32_bf16 v[148:151], v[68:71], v[100:103], v[148:151]\n\t" \
    "ds_read_b64_tr_b16 v[92:93], v203 offset:4096\n\t" \
    "v_mfma_f32_16x16x32_bf16 v[144:147], v[68:71], v[96:99], v[144:147]\n\t" \
    "ds_read_b64_tr_b16 v[94:95], v203 offset:4608\n\t" \
    "buffer_load_dwordx4 v215, %5, %3 offen offset:3072 lds\n\t" \
    "v_mfma_f32_16x16x32_bf16 v[160:163], v[72:75], v[96:99], v[160:163]\n\t" \
    "ds_read_b128 v[112:115], v196 offset:64\n\t" \
    "v_mfma_f32_16x16x32_bf16 v[164:167], v[72:75], v[100:103], v[164:167]\n\t" \
    "ds_read_b128 v[116:119], v196 offset:16704\n\t" \
    "v_mfma_f32_16x16x32_bf16 v[168:171], v[72:75], v[104:107], v[168:171]\n\t" \
    "ds_read_b128 v[120:123], v196 offset:33344\n\t" \
    "v_mfma_f32_16x16x32_bf16 v[172:175], v[72:75], v[108:111], v[172:175]\n\t" \
    "ds_read_b128 v[124:127], v196 offset:49984\n\t" \
    "v_mfma_f32_16x16x32_bf16 v[188:191], v[76:79], v[108:111], v[188:191]\n\t" \
    "v_mfma_f32_16x16x32_bf16 v[184:187], v[76:79], v[104:107], v[184:187]\n\t" \
    "v_mfma_f32_16x16x32_bf16 v[180:183], v[76:79], v[100:103], v[180:183]\n\t" \
    "v_mfma_f32_16x16x32_bf16 v[176:179], v[76:79], v[96:99], v[176:179]\n\t" \
    "s_waitcnt vmcnt(0) lgkmcnt(0)\n\t" \
    "v_mfma_f32_16x16x32_bf16 v[128:131], v[80:83], v[112:115], v[128:131]\n\t" \
    "ds_read_b64_tr_b16 v[64:65], v200 offset:0\n\t" \
    "v_mfma_f32_16x16x32_bf16 v[132:135], v[80:83], v[116:119], v[132:135]\n\t" \
    "ds_read_b64_tr_b16 v[66:67], v200 offset:512\n\t" \
    "s_add_u32 %4, %6, 22\n\t" \
    "s_and_b32 %4, %4, 31\n\t" \
    "s_add_u32 m0, %9, 4096\n\t" \
    "s_mul_i32 %3, %4, 57344\n\t" \
    "buffer_load_dwordx4 %7, %5, %3 offen offset:0 lds\n\t" \
    "v_mfma_f32_16x16x32_bf16 v[136:139], v[80:83], v[120:123], v[136:139]\n\t" \
    "ds_read_b64_tr_b16 v[68:69], v201 offset:0\n\t" \
    "v_mfma_f32_16x16x32_bf16 v[140:143], v[80:83], v[124:127], v[140:143]\n\t" \
    "ds_read_b64_tr_b16 v[70:71], v201 offset:512\n\t" \
    "buffer_load_dwordx4 v213, %5, %3 offen offset:1024 lds\n\t" \
    "v_mfma_f32_16x16x32_bf16 v[156:159], v[84:87], v[124:127], v[156:159]\n\t" \
    "ds_read_b64_tr_b16 v[72:73], v202 offset:0\n\t" \
    "v_mfma_f32_16x16x32_bf16 v[152:155], v[84:87], v[120:123], v[152:155]\n\t" \
    "ds_read_b64_tr_b16 v[74:75], v202 offset:512\n\t" \
    "buffer_load_dwordx4 v214, %5, %3 offen offset:2048 lds\n\t" \
    "v_mfma_f32_16x16x32_bf16 v[148:151], v[84:87], v[116:119], v[148:151]\n\t" \
    "ds_read_b64_tr_b16 v[76:77], v203 offset:0\n\t" \
    "v_mfma_f32_16x16x32_bf16 v[144:147], v[84:87], v[112:115], v[144:147]\n\t" \
    "ds_read_b64_tr_b16 v[78:79], v203 offset:512\n\t" \
    "buffer_load_dwordx4 v215, %5, %3 offen offset:3072 lds\n\t" \
    "v_mfma_f32_16x16x32_bf16 v[160:163], v[88:91], v[112:115], v[160:163]\n\t" \
    "ds_read_b128 v[96:99], v197 offset:0\n\t" \
    "v_mfma_f32_16x16x32_bf16 v[164:167], v[88:91], v[116:119], v[164:167]\n\t" \
    "ds_read_b128 v[100:103], v197 offset:16640\n\t" \
    "v_mfma_f32_16x16x32_bf16 v[168:171], v[88:91], v[120:123], v[168:171]\n\t" \
    "ds_read_b128 v[104:107], v197 offset:33280\n\t" \
    "v_mfma_f32_16x16x32_bf16 v[172:175], v[88:91], v[124:127], v[172:175]\n\t" \
    "ds_read_b128 v[108:111], v197 offset:49920\n\t" \
    "v_mfma_f32_16x16x32_bf16 v[188:191], v[92:95], v[124:127], v[188:191]\n\t" \
    "v_mfma_f32_16x16x32_bf16 v[184:187], v[92:95], v[120:123], v[184:187]\n\t" \
    "v_mfma_f32_16x16x32_bf16 v[180:183], v[92:95], v[116:119], v[180:183]\n\t" \
    "v_mfma_f32_16x16x32_bf16 v[176:179], v[92:95], v[112:115], v[176:179]\n\t" \
    "s_waitcnt vmcnt(0) lgkmcnt(0)\n\t" \
    "v_mfma_f32_16x16x32_bf16 v[128:131], v[64:67], v[96:99], v[128:131]\n\t" \
    "ds_read_b64_tr_b16 v[80:81], v200 offset:4096\n\t" \
    "v_mfma_f32_16x16x32_bf16 v[132:135], v[64:67], v[100:103], v[132:135]\n\t" \
    "ds_read_b64_tr_b16 v[82:83], v200 offset:4608\n\t" \
    "s_add_u32 %4, %6, 24\n\t" \
    "s_and_b32 %4, %4, 31\n\t" \
    "s_add_u32 m0, %9, 0\n\t" \
    "s_mul_i32 %3, %4, 57344\n\t" \
    "buffer_load_dwordx4 %7, %5, %3 offen offset:0 lds\n\t" \
    "v_mfma_f32_16x16x32_bf16 v[136:139], v[64:67], v[104:107], v[136:139]\n\t" \
    "ds_read_b64_tr_b16 v[84:85], v201 offset:4096\n\t" \
    "v_mfma_f32_16x16x32_bf16 v[140:143], v[64:67], v[108:111], v[140:143]\n\t" \
    "ds_read_b64_tr_b16 v[86:87], v201 offset:4608\n\t" \
    "buffer_load_dwordx4 v213, %5, %3 offen offset:1024 lds\n\t" \
    "v_mfma_f32_16x16x32_bf16 v[156:159], v[68:71], v[108:111], v[156:159]\n\t" \
    "ds_read_b64_tr_b16 v[88:89], v202 offset:4096\n\t" \
    "v_mfma_f32_16x16x32_bf16 v[152:155], v[68:71], v[104:107], v[152:155]\n\t" \
    "ds_read_b64_tr_b16 v[90:91], v202 offset:4608\n\t" \
    "buffer_load_dwordx4 v214, %5, %3 offen offset:2048 lds\n\t" \
    "v_mfma_f32_16x16x32_bf16 v[148:151], v[68:71], v[100:103], v[148:151]\n\t" \
    "ds_read_b64_tr_b16 v[92:93], v203 offset:4096\n\t" \
    "v_mfma_f32_16x16x32_bf16 v[144:147], v[68:71], v[96:99], v[144:147]\n\t" \
    "ds_read_b64_tr_b16 v[94:95], v203 offset:4608\n\t" \
    "buffer_load_dwordx4 v215, %5, %3 offen offset:3072 lds\n\t" \
    "v_mfma_f32_16x16x32_bf16 v[160:163], v[72:75], v[96:99], v[160:163]\n\t" \
    "ds_read_b128 v[112:115], v197 offset:64\n\t" \
    "v_mfma_f32_16x16x32_bf16 v[164:167], v[72:75], v[100:103], v[164:167]\n\t" \
    "ds_read_b128 v[116:119], v197 offset:16704\n\t" \
    "v_mfma_f32_16x16x32_bf16 v[168:171], v[72:75], v[104:107], v[168:171]\n\t" \
    "ds_read_b128 v[120:123], v197 offset:33344\n\t" \
    "v_mfma_f32_16x16x32_bf16 v[172:175], v[72:75], v[108:111], v[172:175]\n\t" \
    "ds_read_b128 v[124:127], v197 offset:49984\n\t" \
    "v_mfma_f32_16x16x32_bf16 v[188:191], v[76:79], v[108:111], v[188:191]\n\t" \
    "v_mfma_f32_16x16x32_bf16 v[184:187], v[76:79], v[104:107], v[184:187]\n\t" \
    "v_mfma_f32_16x16x32_bf16 v[180:183], v[76:79], v[100:103], v[180:183]\n\t" \
    "v_mfma_f32_16x16x32_bf16 v[176:179], v[76:79], v[96:99], v[176:179]\n\t" \
    "s_waitcnt vmcnt(0) lgkmcnt(0)\n\t" \
    "v_mfma_f32_16x16x32_bf16 v[128:131], v[80:83], v[112:115], v[128:131]\n\t" \
    "ds_read_b64_tr_b16 v[64:65], v200 offset:0\n\t" \
    "v_mfma_f32_16x16x32_bf16 v[132:135], v[80:83], v[116:119], v[132:135]\n\t" \
    "ds_read_b64_tr_b16 v[66:67], v200 offset:512\n\t" \
    "s_add_u32 %4, %6, 26\n\t" \
    "s_and_b32 %4, %4, 31\n\t" \
    "s_add_u32 m0, %9, 4096\n\t" \
    "s_mul_i32 %3, %4, 57344\n\t" \
    "buffer_load_dwordx4 %7, %5, %3 offen offset:0 lds\n\t" \
    "v_mfma_f32_16x16x32_bf16 v[136:139], v[80:83], v[120:123], v[136:139]\n\t" \
    "ds_read_b64_tr_b16 v[68:69], v201 offset:0\n\t" \
    "v_mfma_f32_16x16x32_bf16 v[140:143], v[80:83], v[124:127], v[140:143]\n\t" \
    "ds_read_b64_tr_b16 v[70:71], v201 offset:512\n\t" \
    "buffer_load_dwordx4 v213, %5, %3 offen offset:1024 lds\n\t" \
    "v_mfma_f32_16x16x32_bf16 v[156:159], v[84:87], v[124:127], v[156:159]\n\t" \
    "ds_read_b64_tr_b16 v[72:73], v202 offset:0\n\t" \
    "v_mfma_f32_16x16x32_bf16 v[152:155], v[84:87], v[120:123], v[152:155]\n\t" \
    "ds_read_b64_tr_b16 v[74:75], v202 offset:512\n\t" \
    "buffer_load_dwordx4 v214, %5, %3 offen offset:2048 lds\n\t" \
    "v_mfma_f32_16x16x32_bf16 v[148:151], v[84:87], v[116:119], v[148:151]\n\t" \
    "ds_read_b64_tr_b16 v[76:77], v203 offset:0\n\t" \
    "v_mfma_f32_16x16x32_bf16 v[144:147], v[84:87], v[112:115], v[144:147]\n\t" \
    "ds_read_b64_tr_b16 v[78:79], v203 offset:512\n\t" \
    "buffer_load_dwordx4 v215, %5, %3 offen offset:3072 lds\n\t" \
    "v_mfma_f32_16x16x32_bf16 v[160:163], v[88:91], v[112:115], v[160:163]\n\t" \
    "ds_read_b128 v[96:99], v198 offset:0\n\t" \
    "v_mfma_f32_16x16x32_bf16 v[164:167], v[88:91], v[116:119], v[164:167]\n\t" \
    "ds_read_b128 v[100:103], v198 offset:16640\n\t" \
    "v_mfma_f32_16x16x32_bf16 v[168:171], v[88:91], v[120:123], v[168:171]\n\t" \
    "ds_read_b128 v[104:107], v198 offset:33280\n\t" \
    "v_mfma_f32_16x16x32_bf16 v[172:175], v[88:91], v[124:127], v[172:175]\n\t" \
    "ds_read_b128 v[108:111], v198 offset:49920\n\t" \
    "v_mfma_f32_16x16x32_bf16 v[188:191], v[92:95], v[124:127], v[188:191]\n\t" \
    "v_mfma_f32_16x16x32_bf16 v[184:187], v[92:95], v[120:123], v[184:187]\n\t" \
    "v_mfma_f32_16x16x32_bf16 v[180:183], v[92:95], v[116:119], v[180:183]\n\t" \
    "v_mfma_f32_16x16x32_bf16 v[176:179], v[92:95], v[112:115], v[176:179]\n\t" \
    "s_waitcnt vmcnt(0) lgkmcnt(0)\n\t" \
    "v_mfma_f32_16x16x32_bf16 v[128:131], v[64:67], v[96:99], v[128:131]\n\t" \
    "ds_read_b64_tr_b16 v[80:81], v200 offset:4096\n\t" \
    "v_mfma_f32_16x16x32_bf16 v[132:135], v[64:67], v[100:103], v[132:135]\n\t" \
    "ds_read_b64_tr_b16 v[82:83], v200 offset:4608\n\t" \
    "s_add_u32 %4, %6, 28\n\t" \
    "s_and_b32 %4, %4, 31\n\t" \
    "s_add_u32 m0, %9, 0\n\t" \
    "s_mul_i32 %3, %4, 57344\n\t" \
    "buffer_load_dwordx4 %7, %5, %3 offen offset:0 lds\n\t" \
    "v_mfma_f32_16x16x32_bf16 v[136:139], v[64:67], v[104:107], v[136:139]\n\t" \
    "ds_read_b64_tr_b16 v[84:85], v201 offset:4096\n\t" \
    "v_mfma_f32_16x16x32_bf16 v[140:143], v[64:67], v[108:111], v[140:143]\n\t" \
    "ds_read_b64_tr_b16 v[86:87], v201 offset:4608\n\t" \
    "buffer_load_dwordx4 v213, %5, %3 offen offset:1024 lds\n\t" \
    "v_mfma_f32_16x16x32_bf16 v[156:159], v[68:71], v[108:111], v[156:159]\n\t" \
    "ds_read_b64_tr_b16 v[88:89], v202 offset:4096\n\t" \
    "v_mfma_f32_16x16x32_bf16 v[152:155], v[68:71], v[104:107], v[152:155]\n\t" \
    "ds_read_b64_tr_b16 v[90:91], v202 offset:4608\n\t" \
    "buffer_load_dwordx4 v214, %5, %3 offen offset:2048 lds\n\t" \
    "v_mfma_f32_16x16x32_bf16 v[148:151], v[68:71], v[100:103], v[148:151]\n\t" \
    "ds_read_b64_tr_b16 v[92:93], v203 offset:4096\n\t" \
    "v_mfma_f32_16x16x32_bf16 v[144:147], v[68:71], v[96:99], v[144:147]\n\t" \
    "ds_read_b64_tr_b16 v[94:95], v203 offset:4608\n\t" \
    "buffer_load_dwordx4 v215, %5, %3 offen offset:3072 lds\n\t" \
    "v_mfma_f32_16x16x32_bf16 v[160:163], v[72:75], v[96:99], v[160:163]\n\t" \
    "ds_read_b128 v[112:115], v198 offset:64\n\t" \
    "v_mfma_f32_16x16x32_bf16 v[164:167], v[72:75], v[100:103], v[164:167]\n\t" \
    "ds_read_b128 v[116:119], v198 offset:16704\n\t" \
    "v_mfma_f32_16x16x32_bf16 v[168:171], v[72:75], v[104:107], v[168:171]\n\t" \
    "ds_read_b128 v[120:123], v198 offset:33344\n\t" \
    "v_mfma_f32_16x16x32_bf16 v[172:175], v[72:75], v[108:111], v[172:175]\n\t" \
    "ds_read_b128 v[124:127], v198 offset:49984\n\t" \
    "v_mfma_f32_16x16x32_bf16 v[188:191], v[76:79], v[108:111], v[188:191]\n\t" \
    "v_mfma_f32_16x16x32_bf16 v[184:187], v[76:79], v[104:107], v[184:187]\n\t" \
    "v_mfma_f32_16x16x32_bf16 v[180:183], v[76:79], v[100:103], v[180:183]\n\t" \
    "v_mfma_f32_16x16x32_bf16 v[176:179], v[76:79], v[96:99], v[176:179]\n\t" \
    "s_waitcnt vmcnt(0) lgkmcnt(0)\n\t" \
    "v_mfma_f32_16x16x32_bf16 v[128:131], v[80:83], v[112:115], v[128:131]\n\t" \
    "ds_read_b64_tr_b16 v[64:65], v200 offset:0\n\t" \
    "v_mfma_f32_16x16x32_bf16 v[132:135], v[80:83], v[116:119], v[132:135]\n\t" \
    "ds_read_b64_tr_b16 v[66:67], v200 offset:512\n\t" \
    "s_add_u32 %4, %6, 30\n\t" \
    "s_and_b32 %4, %4, 31\n\t" \
    "s_add_u32 m0, %9, 4096\n\t" \
    "s_mul_i32 %3, %4, 57344\n\t" \
    "buffer_load_dwordx4 %7, %5, %3 offen offset:0 lds\n\t" \
    "v_mfma_f32_16x16x32_bf16 v[136:139], v[80:83], v[120:123], v[136:139]\n\t" \
    "ds_read_b64_tr_b16 v[68:69], v201 offset:0\n\t" \
    "v_mfma_f32_16x16x32_bf16 v[140:143], v[80:83], v[124:127], v[140:143]\n\t" \
    "ds_read_b64_tr_b16 v[70:71], v201 offset:512\n\t" \
    "buffer_load_dwordx4 v213, %5, %3 offen offset:1024 lds\n\t" \
    "v_mfma_f32_16x16x32_bf16 v[156:159], v[84:87], v[124:127], v[156:159]\n\t" \
    "ds_read_b64_tr_b16 v[72:73], v202 offset:0\n\t" \
    "v_mfma_f32_16x16x32_bf16 v[152:155], v[84:87], v[120:123], v[152:155]\n\t" \
    "ds_read_b64_tr_b16 v[74:75], v202 offset:512\n\t" \
    "buffer_load_dwordx4 v214, %5, %3 offen offset:2048 lds\n\t" \
    "v_mfma_f32_16x16x32_bf16 v[148:151], v[84:87], v[116:119], v[148:151]\n\t" \
    "ds_read_b64_tr_b16 v[76:77], v203 offset:0\n\t" \
    "v_mfma_f32_16x16x32_bf16 v[144:147], v[84:87], v[112:115], v[144:147]\n\t" \
    "ds_read_b64_tr_b16 v[78:79], v203 offset:512\n\t" \
    "buffer_load_dwordx4 v215, %5, %3 offen offset:3072 lds\n\t" \
    "v_mfma_f32_16x16x32_bf16 v[160:163], v[88:91], v[112:115], v[160:163]\n\t" \
    "ds_read_b128 v[96:99], v199 offset:0\n\t" \
    "v_mfma_f32_16x16x32_bf16 v[164:167], v[88:91], v[116:119], v[164:167]\n\t" \
    "ds_read_b128 v[100:103], v199 offset:16640\n\t" \
    "v_mfma_f32_16x16x32_bf16 v[168:171], v[88:91], v[120:123], v[168:171]\n\t" \
    "ds_read_b128 v[104:107], v199 offset:33280\n\t" \
    "v_mfma_f32_16x16x32_bf16 v[172:175], v[88:91], v[124:127], v[172:175]\n\t" \
    "ds_read_b128 v[108:111], v199 offset:49920\n\t" \
    "v_mfma_f32_16x16x32_bf16 v[188:191], v[92:95], v[124:127], v[188:191]\n\t" \
    "v_mfma_f32_16x16x32_bf16 v[184:187], v[92:95], v[120:123], v[184:187]\n\t" \
    "v_mfma_f32_16x16x32_bf16 v[180:183], v[92:95], v[116:119], v[180:183]\n\t" \
    "v_mfma_f32_16x16x32_bf16 v[176:179], v[92:95], v[112:115], v[176:179]\n\t" \
    "s_waitcnt vmcnt(0) lgkmcnt(0)\n\t" \
    "v_mfma_f32_16x16x32_bf16 v[128:131], v[64:67], v[96:99], v[128:131]\n\t" \
    "ds_read_b64_tr_b16 v[80:81], v200 offset:4096\n\t" \
    "v_mfma_f32_16x16x32_bf16 v[132:135], v[64:67], v[100:103], v[132:135]\n\t" \
    "ds_read_b64_tr_b16 v[82:83], v200 offset:4608\n\t" \
    "v_mfma_f32_16x16x32_bf16 v[136:139], v[64:67], v[104:107], v[136:139]\n\t" \
    "ds_read_b64_tr_b16 v[84:85], v201 offset:4096\n\t" \
    "v_mfma_f32_16x16x32_bf16 v[140:143], v[64:67], v[108:111], v[140:143]\n\t" \
    "ds_read_b64_tr_b16 v[86:87], v201 offset:4608\n\t" \
    "v_mfma_f32_16x16x32_bf16 v[156:159], v[68:71], v[108:111], v[156:159]\n\t" \
    "ds_read_b64_tr_b16 v[88:89], v202 offset:4096\n\t" \
    "v_mfma_f32_16x16x32_bf16 v[152:155], v[68:71], v[104:107], v[152:155]\n\t" \
    "ds_read_b64_tr_b16 v[90:91], v202 offset:4608\n\t" \
    "v_mfma_f32_16x16x32_bf16 v[148:151], v[68:71], v[100:103], v[148:151]\n\t" \
    "ds_read_b64_tr_b16 v[92:93], v203 offset:4096\n\t" \
    "v_mfma_f32_16x16x32_bf16 v[144:147], v[68:71], v[96:99], v[144:147]\n\t" \
    "ds_read_b64_tr_b16 v[94:95], v203 offset:4608\n\t" \
    "v_mfma_f32_16x16x32_bf16 v[160:163], v[72:75], v[96:99], v[160:163]\n\t" \
    "ds_read_b128 v[112:115], v199 offset:64\n\t" \
    "v_mfma_f32_16x16x32_bf16 v[164:167], v[72:75], v[100:103], v[164:167]\n\t" \
    "ds_read_b128 v[116:119], v199 offset:16704\n\t" \
    "v_mfma_f32_16x16x32_bf16 v[168:171], v[72:75], v[104:107], v[168:171]\n\t" \
    "ds_read_b128 v[120:123], v199 offset:33344\n\t" \
    "v_mfma_f32_16x16x32_bf16 v[172:175], v[72:75], v[108:111], v[172:175]\n\t" \
    "ds_read_b128 v[124:127], v199 offset:49984\n\t" \
    "v_mfma_f32_16x16x32_bf16 v[188:191], v[76:79], v[108:111], v[188:191]\n\t" \
    "v_mfma_f32_16x16x32_bf16 v[184:187], v[76:79], v[104:107], v[184:187]\n\t" \
    "v_mfma_f32_16x16x32_bf16 v[180:183], v[76:79], v[100:103], v[180:183]\n\t" \
    "v_mfma_f32_16x16x32_bf16 v[176:179], v[76:79], v[96:99], v[176:179]\n\t" \
    "s_waitcnt lgkmcnt(0)\n\t" \
    "v_mfma_f32_16x16x32_bf16 v[128:131], v[80:83], v[112:115], v[128:131]\n\t" \
    "v_mfma_f32_16x16x32_bf16 v[132:135], v[80:83], v[116:119], v[132:135]\n\t" \
    "v_mfma_f32_16x16x32_bf16 v[136:139], v[80:83], v[120:123], v[136:139]\n\t" \
    "v_mfma_f32_16x16x32_bf16 v[140:143], v[80:83], v[124:127], v[140:143]\n\t" \
    "v_mfma_f32_16x16x32_bf16 v[156:159], v[84:87], v[124:127], v[156:159]\n\t" \
    "v_mfma_f32_16x16x32_bf16 v[152:155], v[84:87], v[120:123], v[152:155]\n\t" \
    "v_mfma_f32_16x16x32_bf16 v[148:151], v[84:87], v[116:119], v[148:151]\n\t" \
    "v_mfma_f32_16x16x32_bf16 v[144:147], v[84:87], v[112:115], v[144:147]\n\t" \
    "v_mfma_f32_16x16x32_bf16 v[160:163], v[88:91], v[112:115], v[160:163]\n\t" \
    "v_mfma_f32_16x16x32_bf16 v[164:167], v[88:91], v[116:119], v[164:167]\n\t" \
    "v_mfma_f32_16x16x32_bf16 v[168:171], v[88:91], v[120:123], v[168:171]\n\t" \
    "v_mfma_f32_16x16x32_bf16 v[172:175], v[88:91], v[124:127], v[172:175]\n\t" \
    "v_mfma_f32_16x16x32_bf16 v[188:191], v[92:95], v[124:127], v[188:191]\n\t" \
    "v_mfma_f32_16x16x32_bf16 v[184:187], v[92:95], v[120:123], v[184:187]\n\t" \
    "v_mfma_f32_16x16x32_bf16 v[180:183], v[92:95], v[116:119], v[180:183]\n\t" \
    "v_mfma_f32_16x16x32_bf16 v[176:179], v[92:95], v[112:115], v[176:179]\n\t" \
    "s_setprio 2\n\t" \
    "s_add_u32 %3, %13, 24\n\t" \
    "s_mov_b32 %4, 0\n\t" \
    "9:\n\t" \
    "v_mov_b32 v208, %3\n\t" \
    "ds_read_b32 v208, v208\n\t" \
    "s_waitcnt lgkmcnt(0)\n\t" \
    "v_readfirstlane_b32 vcc_lo, v208\n\t" \
    "s_add_u32 %4, %4, 1\n\t" \
    "s_cmp_ge_u32 vcc_lo, 4\n\t" \
    "s_cbranch_scc1 90f\n\t" \
    "s_sleep 1\n\t" \
    "s_cmp_lt_u32 %4, 0x400000\n\t" \
    "s_cbranch_scc1 9b\n\t" \
    "90:\n\t" \
    "v_add_u32 v213, 7168, %15\n\t" \
    "v_add_u32 v217, 7168, %23\n\t" \
    "v_add_u32 v214, 14336, %15\n\t" \
    "v_add_u32 v218, 14336, %23\n\t" \
    "v_add_u32 v215, 21504, %15\n\t" \
    "v_add_u32 v219, 21504, %23\n\t" \
    "s_add_u32 %3, %16, 0\n\t" \
    "s_add_u32 m0, %9, 0\n\t" \
    "s_lshl_b32 %3, %3, 15\n\t" \
    "buffer_load_dwordx4 %15, %14, %3 offen offset:0 lds\n\t" \
    "buffer_load_dwordx4 v213, %14, %3 offen offset:1024 lds\n\t" \
    "buffer_load_dwordx4 v214, %14, %3 offen offset:2048 lds\n\t" \
    "buffer_load_dwordx4 v215, %14, %3 offen offset:3072 lds\n\t" \
    "s_add_u32 %3, %16, 1\n\t" \
    "s_add_u32 m0, %9, 4096\n\t" \
    "s_lshl_b32 %3, %3, 15\n\t" \
    "buffer_load_dwordx4 %15, %14, %3 offen offset:0 lds\n\t" \
    "buffer_load_dwordx4 v213, %14, %3 offen offset:1024 lds\n\t" \
    "buffer_load_dwordx4 v214, %14, %3 offen offset:2048 lds\n\t" \
    "buffer_load_dwordx4 v215, %14, %3 offen offset:3072 lds\n\t" \
    "s_add_u32 m0, %10, 0\n\t" \
    "s_lshl_b32 %3, %21, 17\n\t" \
    "s_add_u32 %3, %3, 0\n\t" \
    "buffer_load_dwordx4 %23, %22, %3 offen offset:0 lds\n\t" \
    "buffer_load_dwordx4 v217, %22, %3 offen offset:1024 lds\n\t" \
    "buffer_load_dwordx4 v218, %22, %3 offen offset:2048 lds\n\t" \
    "buffer_load_dwordx4 v219, %22, %3 offen offset:3072 lds\n\t" \
    "s_add_u32 m0, %10, 4096\n\t" \
    "s_lshl_b32 %3, %21, 17\n\t" \
    "s_add_u32 %3, %3, 32768\n\t" \
    "buffer_load_dwordx4 %23, %22, %3 offen offset:0 lds\n\t" \
    "buffer_load_dwordx4 v217, %22, %3 offen offset:1024 lds\n\t" \
    "buffer_load_dwordx4 v218, %22, %3 offen offset:2048 lds\n\t" \
    "buffer_load_dwordx4 v219, %22, %3 offen offset:3072 lds\n\t" \
    "s_add_u32 %4, %21, 0\n\t" \
    "s_and_b32 %4, %4, 3\n\t" \
    "s_lshl_b32 %4, %4, 5\n\t" \
    "v_xor_b32 v224, %4, %20\n\t" \
    "v_add_u32 v224, %9, v224\n\t" \
    "v_add_u32 v244, %4, %24\n\t" \
    "s_add_u32 %4, %21, 1\n\t" \
    "s_and_b32 %4, %4, 3\n\t" \
    "s_lshl_b32 %4, %4, 5\n\t" \
    "v_xor_b32 v225, %4, %20\n\t" \
    "v_add_u32 v225, %9, v225\n\t" \
    "v_add_u32 v245, %4, %24\n\t" \
    "s_add_u32 %4, %21, 2\n\t" \
    "s_and_b32 %4, %4, 3\n\t" \
    "s_lshl_b32 %4, %4, 5\n\t" \
    "v_xor_b32 v226, %4, %20\n\t" \
    "v_add_u32 v226, %9, v226\n\t" \
    "v_add_u32 v246, %4, %24\n\t" \
    "s_add_u32 %4, %21, 3\n\t" \
    "s_and_b32 %4, %4, 3\n\t" \
    "s_lshl_b32 %4, %4, 5\n\t" \
    "v_xor_b32 v227, %4, %20\n\t" \
    "v_add_u32 v227, %9, v227\n\t" \
    "v_add_u32 v247, %4, %24\n\t" \
    "v_cvt_pk_bf16_f32 v192, v128, v129\n\t" \
    "v_cvt_pk_bf16_f32 v193, v130, v131\n\t" \
    "v_cvt_pk_bf16_f32 v194, v144, v145\n\t" \
    "v_cvt_pk_bf16_f32 v195, v146, v147\n\t" \
    "v_pk_max_i16 v192, v192, 0\n\t" \
    "v_pk_max_i16 v193, v193, 0\n\t" \
    "v_pk_max_i16 v194, v194, 0\n\t" \
    "v_pk_max_i16 v195, v195, 0\n\t" \
    "buffer_store_dwordx4 v[192:195], %18, %17, %19 offen offset:0 sc1\n\t" \
    "v_cvt_pk_bf16_f32 v196, v132, v133\n\t" \
    "v_cvt_pk_bf16_f32 v197, v134, v135\n\t" \
    "v_cvt_pk_bf16_f32 v198, v148, v149\n\t" \
    "v_cvt_pk_bf16_f32 v199, v150, v151\n\t" \
    "v_pk_max_i16 v196, v196, 0\n\t" \
    "v_pk_max_i16 v197, v197, 0\n\t" \
    "v_pk_max_i16 v198, v198, 0\n\t" \
    "v_pk_max_i16 v199, v199, 0\n\t" \
    "buffer_store_dwordx4 v[196:199], %18, %17, %19 offen offset:1024 sc1\n\t" \
    "v_cvt_pk_bf16_f32 v200, v136, v137\n\t" \
    "v_cvt_pk_bf16_f32 v201, v138, v139\n\t" \
    "v_cvt_pk_bf16_f32 v202, v152, v153\n\t" \
    "v_cvt_pk_bf16_f32 v203, v154, v155\n\t" \
    "v_pk_max_i16 v200, v200, 0\n\t" \
    "v_pk_max_i16 v201, v201, 0\n\t" \
    "v_pk_max_i16 v202, v202, 0\n\t" \
    "v_pk_max_i16 v203, v203, 0\n\t" \
    "buffer_store_dwordx4 v[200:203], %18, %17, %19 offen offset:2048 sc1\n\t" \
    "v_cvt_pk_bf16_f32 v204, v140, v141\n\t" \
    "v_cvt_pk_bf16_f32 v205, v142, v143\n\t" \
    "v_cvt_pk_bf16_f32 v206, v156, v157\n\t" \
    "v_cvt_pk_bf16_f32 v207, v158, v159\n\t" \
    "v_pk_max_i16 v204, v204, 0\n\t" \
    "v_pk_max_i16 v205, v205, 0\n\t" \
    "v_pk_max_i16 v206, v206, 0\n\t" \
    "v_pk_max_i16 v207, v207, 0\n\t" \
    "buffer_store_dwordx4 v[204:207], %18, %17, %19 offen offset:3072 sc1\n\t" \
    "s_add_u32 %4, %19, 4096\n\t" \
    "v_cvt_pk_bf16_f32 v208, v160, v161\n\t" \
    "v_cvt_pk_bf16_f32 v209, v162, v163\n\t" \
    "v_cvt_pk_bf16_f32 v210, v176, v177\n\t" \
    "v_cvt_pk_bf16_f32 v211, v178, v179\n\t" \
    "v_pk_max_i16 v208, v208, 0\n\t" \
    "v_pk_max_i16 v209, v209, 0\n\t" \
    "v_pk_max_i16 v210, v210, 0\n\t" \
    "v_pk_max_i16 v211, v211, 0\n\t" \
    "buffer_store_dwordx4 v[208:211], %18, %17, %4 offen offset:0 sc1\n\t" \
    "v_cvt_pk_bf16_f32 v212, v164, v165\n\t" \
    "v_cvt_pk_bf16_f32 v213, v166, v167\n\t" \
    "v_cvt_pk_bf16_f32 v214, v180, v181\n\t" \
    "v_cvt_pk_bf16_f32 v215, v182, v183\n\t" \
    "v_pk_max_i16 v212, v212, 0\n\t" \
    "v_pk_max_i16 v213, v213, 0\n\t" \
    "v_pk_max_i16 v214, v214, 0\n\t" \
    "v_pk_max_i16 v215, v215, 0\n\t" \
    "buffer_store_dwordx4 v[212:215], %18, %17, %4 offen offset:1024 sc1\n\t" \
    "v_cvt_pk_bf16_f32 v216, v168, v169\n\t" \
    "v_cvt_pk_bf16_f32 v217, v170, v171\n\t" \
    "v_cvt_pk_bf16_f32 v218, v184, v185\n\t" \
    "v_cvt_pk_bf16_f32 v219, v186, v187\n\t" \
    "v_pk_max_i16 v216, v216, 0\n\t" \
    "v_pk_max_i16 v217, v217, 0\n\t" \
    "v_pk_max_i16 v218, v218, 0\n\t" \
    "v_pk_max_i16 v219, v219, 0\n\t" \
    "buffer_store_dwordx4 v[216:219], %18, %17, %4 offen offset:2048 sc1\n\t" \
    "v_cvt_pk_bf16_f32 v220, v172, v173\n\t" \
    "v_cvt_pk_bf16_f32 v221, v174, v175\n\t" \
    "v_cvt_pk_bf16_f32 v222, v188, v189\n\t" \
    "v_cvt_pk_bf16_f32 v223, v190, v191\n\t" \
    "v_pk_max_i16 v220, v220, 0\n\t" \
    "v_pk_max_i16 v221, v221, 0\n\t" \
    "v_pk_max_i16 v222, v222, 0\n\t" \
    "v_pk_max_i16 v223, v223, 0\n\t" \
    "buffer_store_dwordx4 v[220:223], %18, %17, %4 offen offset:3072 sc1\n\t" \
    "s_waitcnt vmcnt(8)\n\t" \
    "s_setprio 2\n\t" \
    "ds_read_b64_tr_b16 v[32:33], v244 offset:0\n\t" \
    "ds_read_b64_tr_b16 v[34:35], v244 offset:1024\n\t" \
    "ds_read_b64_tr_b16 v[36:37], v245 offset:0\n\t" \
    "ds_read_b64_tr_b16 v[38:39], v245 offset:1024\n\t" \
    "ds_read_b64_tr_b16 v[40:41], v246 offset:0\n\t" \
    "ds_read_b64_tr_b16 v[42:43], v246 offset:1024\n\t" \
    "ds_read_b64_tr_b16 v[44:45], v247 offset:0\n\t" \
    "ds_read_b64_tr_b16 v[46:47], v247 offset:1024\n\t" \
    "ds_read_b64_tr_b16 v[48:49], v244 offset:4096\n\t" \
    "ds_read_b64_tr_b16 v[50:51], v244 offset:5120\n\t" \
    "ds_read_b64_tr_b16 v[52:53], v245 offset:4096\n\t" \
    "ds_read_b64_tr_b16 v[54:55], v245 offset:5120\n\t" \
    "ds_read_b64_tr_b16 v[56:57], v246 offset:4096\n\t" \
    "ds_read_b64_tr_b16 v[58:59], v246 offset:5120\n\t" \
    "ds_read_b64_tr_b16 v[60:61], v247 offset:4096\n\t" \
    "ds_read_b64_tr_b16 v[62:63], v247 offset:5120\n\t" \
    "ds_read_b64_tr_b16 v[228:229], v224 offset:0\n\t" \
    "ds_read_b64_tr_b16 v[230:231], v224 offset:2048\n\t" \
    "ds_read_b64_tr_b16 v[232:233], v225 offset:0\n\t" \
    "ds_read_b64_tr_b16 v[234:235], v225 offset:2048\n\t" \
    "ds_read_b64_tr_b16 v[236:237], v226 offset:0\n\t" \
    "ds_read_b64_tr_b16 v[238:239], v226 offset:2048\n\t" \
    "ds_read_b64_tr_b16 v[240:241], v227 offset:0\n\t" \
    "ds_read_b64_tr_b16 v[242:243], v227 offset:2048\n\t" \
    "s_waitcnt lgkmcnt(6)\n\t" \
    "v_mfma_f32_16x16x32_bf16 v[64:67], v[228:231], v[192:195], 0\n\t" \
    "v_mfma_f32_16x16x32_bf16 v[68:71], v[228:231], v[196:199], 0\n\t" \
    "v_mfma_f32_16x16x32_bf16 v[72:75], v[228:231], v[200:203], 0\n\t" \
    "v_mfma_f32_16x16x32_bf16 v[76:79], v[228:231], v[204:207], 0\n\t" \
    "s_waitcnt lgkmcnt(4)\n\t" \
    "v_mfma_f32_16x16x32_bf16 v[92:95], v[232:235], v[204:207], 0\n\t" \
    "v_mfma_f32_16x16x32_bf16 v[88:91], v[232:235], v[200:203], 0\n\t" \
    "v_mfma_f32_16x16x32_bf16 v[84:87], v[232:235], v[196:199], 0\n\t" \
    "v_mfma_f32_16x16x32_bf16 v[80:83], v[232:235], v[192:195], 0\n\t" \
    "s_waitcnt lgkmcnt(2)\n\t" \
    "v_mfma_f32_16x16x32_bf16 v[96:99], v[236:239], v[192:195], 0\n\t" \
    "v_mfma_f32_16x16x32_bf16 v[100:103], v[236:239], v[196:199], 0\n\t" \
    "v_mfma_f32_16x16x32_bf16 v[104:107], v[236:239], v[200:203], 0\n\t" \
    "v_mfma_f32_16x16x32_bf16 v[108:111], v[236:239], v[204:207], 0\n\t" \
    "s_waitcnt lgkmcnt(0)\n\t" \
    "v_mfma_f32_16x16x32_bf16 v[124:127], v[240:243], v[204:207], 0\n\t" \
    "v_mfma_f32_16x16x32_bf16 v[120:123], v[240:243], v[200:203], 0\n\t" \
    "v_mfma_f32_16x16x32_bf16 v[116:119], v[240:243], v[196:199], 0\n\t" \
    "v_mfma_f32_16x16x32_bf16 v[112:115], v[240:243], v[192:195], 0\n\t" \
    "ds_read_b64_tr_b16 v[228:229], v224 offset:4096\n\t" \
    "ds_read_b64_tr_b16 v[230:231], v224 offset:6144\n\t" \
    "ds_read_b64_tr_b16 v[232:233], v225 offset:4096\n\t" \
    "ds_read_b64_tr_b16 v[234:235], v225 offset:6144\n\t" \
    "ds_read_b64_tr_b16 v[236:237], v226 offset:4096\n\t" \
    "ds_read_b64_tr_b16 v[238:239], v226 offset:6144\n\t" \
    "ds_read_b64_tr_b16 v[240:241], v227 offset:4096\n\t" \
    "ds_read_b64_tr_b16 v[242:243], v227 offset:6144\n\t" \
    "s_waitcnt lgkmcnt(6)\n\t" \
    "v_mfma_f32_16x16x32_bf16 v[64:67], v[228:231], v[208:211], v[64:67]\n\t" \
    "v_mfma_f32_16x16x32_bf16 v[68:71], v[228:231], v[212:215], v[68:71]\n\t" \
    "v_mfma_f32_16x16x32_bf16 v[72:75], v[228:231], v[216:219], v[72:75]\n\t" \
    "v_mfma_f32_16x16x32_bf16 v[76:79], v[228:231], v[220:223], v[76:79]\n\t" \
    "s_waitcnt lgkmcnt(4)\n\t" \
    "v_mfma_f32_16x16x32_bf16 v[92:95], v[232:235], v[220:223], v[92:95]\n\t" \
    "v_mfma_f32_16x16x32_bf16 v[88:91], v[232:235], v[216:219], v[88:91]\n\t" \
    "v_mfma_f32_16x16x32_bf16 v[84:87], v[232:235], v[212:215], v[84:87]\n\t" \
    "v_mfma_f32_16x16x32_bf16 v[80:83], v[232:235], v[208:211], v[80:83]\n\t" \
    "s_waitcnt lgkmcnt(2)\n\t" \
    "v_mfma_f32_16x16x32_bf16 v[96:99], v[236:239], v[208:211], v[96:99]\n\t" \
    "v_mfma_f32_16x16x32_bf16 v[100:103], v[236:239], v[212:215], v[100:103]\n\t" \
    "v_mfma_f32_16x16x32_bf16 v[104:107], v[236:239], v[216:219], v[104:107]\n\t" \
    "v_mfma_f32_16x16x32_bf16 v[108:111], v[236:239], v[220:223], v[108:111]\n\t" \
    "s_waitcnt lgkmcnt(0)\n\t" \
    "v_mfma_f32_16x16x32_bf16 v[124:127], v[240:243], v[220:223], v[124:127]\n\t" \
    "v_mfma_f32_16x16x32_bf16 v[120:123], v[240:243], v[216:219], v[120:123]\n\t" \
    "v_mfma_f32_16x16x32_bf16 v[116:119], v[240:243], v[212:215], v[116:119]\n\t" \
    "v_mfma_f32_16x16x32_bf16 v[112:115], v[240:243], v[208:211], v[112:115]\n\t" \
    "s_setprio 2\n\t" \
    "s_nop 7\n\t" \
    "s_branch 92f\n\t" \
    "91:\n\t" \
    "v_add_u32 v213, 56320, %7\n\t" \
    "s_add_u32 %4, %6, 0\n\t" \
    "s_and_b32 %4, %4, 31\n\t" \
    "s_lshl_b32 %4, %4, 5\n\t" \
    "v_add_u32 v192, %4, %12\n\t" \
    "s_add_u32 %4, %6, 4\n\t" \
    "s_and_b32 %4, %4, 31\n\t" \
    "s_lshl_b32 %4, %4, 5\n\t" \
    "v_add_u32 v193, %4, %12\n\t" \
    "s_add_u32 %4, %6, 8\n\t" \
    "s_and_b32 %4, %4, 31\n\t" \
    "s_lshl_b32 %4, %4, 5\n\t" \
    "v_add_u32 v194, %4, %12\n\t" \
    "s_add_u32 %4, %6, 12\n\t" \
    "s_and_b32 %4, %4, 31\n\t" \
    "s_lshl_b32 %4, %4, 5\n\t" \
    "v_add_u32 v195, %4, %12\n\t" \
    "s_add_u32 %4, %6, 16\n\t" \
    "s_and_b32 %4, %4, 31\n\t" \
    "s_lshl_b32 %4, %4, 5\n\t" \
    "v_add_u32 v196, %4, %12\n\t" \
    "s_add_u32 %4, %6, 20\n\t" \
    "s_and_b32 %4, %4, 31\n\t" \
    "s_lshl_b32 %4, %4, 5\n\t" \
    "v_add_u32 v197, %4, %12\n\t" \
    "s_add_u32 %4, %6, 24\n\t" \
    "s_and_b32 %4, %4, 31\n\t" \
    "s_lshl_b32 %4, %4, 5\n\t" \
    "v_add_u32 v198, %4, %12\n\t" \
    "s_add_u32 %4, %6, 28\n\t" \
    "s_and_b32 %4, %4, 31\n\t" \
    "s_lshl_b32 %4, %4, 5\n\t" \
    "v_add_u32 v199, %4, %12\n\t" \
    "v_xor_b32 v208, 0, %11\n\t" \
    "v_add_u32 v200, %9, v208\n\t" \
    "v_add_u32 v204, %10, v208\n\t" \
    "v_xor_b32 v208, 32, %11\n\t" \
    "v_add_u32 v201, %9, v208\n\t" \
    "v_add_u32 v205, %10, v208\n\t" \
    "s_mov_b32 %4, 0\n\t" \
    "7:\n\t" \
    "v_mov_b32 v208, %13\n\t" \
    "ds_read_b32 v208, v208\n\t" \
    "s_waitcnt lgkmcnt(0)\n\t" \
    "v_readfirstlane_b32 vcc_lo, v208\n\t" \
    "s_add_u32 %4, %4, 1\n\t" \
    "s_cmp_ge_u32 vcc_lo, 7\n\t" \
    "s_cbranch_scc1 70f\n\t" \
    "s_sleep 1\n\t" \
    "s_cmp_lt_u32 %4, 0x400000\n\t" \
    "s_cbranch_scc1 7b\n\t" \
    "70:\n\t" \
    "s_setprio 2\n\t" \
    "s_waitcnt vmcnt(2)\n\t" \
    "ds_read_b64_tr_b16 v[64:65], v200 offset:0\n\t" \
    "ds_read_b64_tr_b16 v[66:67], v200 offset:256\n\t" \
    "ds_read_b64_tr_b16 v[68:69], v201 offset:0\n\t" \
    "ds_read_b64_tr_b16 v[70:71], v201 offset:256\n\t" \
    "ds_read_b128 v[96:99], v192 offset:0\n\t" \
    "ds_read_b128 v[100:103], v192 offset:16640\n\t" \
    "ds_read_b128 v[104:107], v192 offset:33280\n\t" \
    "ds_read_b128 v[108:111], v192 offset:49920\n\t" \
    "s_waitcnt vmcnt(0) lgkmcnt(0)\n\t" \
    "v_mfma_f32_16x16x32_bf16 v[128:131], v[64:67], v[96:99], 0\n\t" \
    "ds_read_b64_tr_b16 v[80:81], v200 offset:2048\n\t" \
    "ds_read_b64_tr_b16 v[82:83], v200 offset:2304\n\t" \
    "v_mfma_f32_16x16x32_bf16 v[132:135], v[64:67], v[100:103], 0\n\t" \
    "ds_read_b64_tr_b16 v[84:85], v201 offset:2048\n\t" \
    "ds_read_b64_tr_b16 v[86:87], v201 offset:2304\n\t" \
    "s_add_u32 %4, %6, 4\n\t" \
    "s_and_b32 %4, %4, 31\n\t" \
    "s_add_u32 m0, %9, 0\n\t" \
    "s_mul_i32 %3, %4, 57344\n\t" \
    "buffer_load_dwordx4 %7, %5, %3 offen offset:0 lds\n\t" \
    "v_mfma_f32_16x16x32_bf16 v[136:139], v[64:67], v[104:107], 0\n\t" \
    "ds_read_b128 v[112:115], v192 offset:64\n\t" \
    "ds_read_b128 v[116:119], v192 offset:16704\n\t" \
    "v_mfma_f32_16x16x32_bf16 v[140:143], v[64:67], v[108:111], 0\n\t" \
    "ds_read_b128 v[120:123], v192 offset:33344\n\t" \
    "ds_read_b128 v[124:127], v192 offset:49984\n\t" \
    "buffer_load_dwordx4 v213, %5, %3 offen offset:1024 lds\n\t" \
    "v_mfma_f32_16x16x32_bf16 v[156:159], v[68:71], v[108:111], 0\n\t" \
    "v_mfma_f32_16x16x32_bf16 v[152:155], v[68:71], v[104:107], 0\n\t" \
    "v_mfma_f32_16x16x32_bf16 v[148:151], v[68:71], v[100:103], 0\n\t" \
    "v_mfma_f32_16x16x32_bf16 v[144:147], v[68:71], v[96:99], 0\n\t" \
    "s_waitcnt vmcnt(0) lgkmcnt(0)\n\t" \
    "v_mfma_f32_16x16x32_bf16 v[128:131], v[80:83], v[112:115], v[128:131]\n\t" \
    "ds_read_b64_tr_b16 v[64:65], v200 offset:0\n\t" \
    "ds_read_b64_tr_b16 v[66:67], v200 offset:256\n\t" \
    "v_mfma_f32_16x16x32_bf16 v[132:135], v[80:83], v[116:119], v[132:135]\n\t" \
    "ds_read_b64_tr_b16 v[68:69], v201 offset:0\n\t" \
    "ds_read_b64_tr_b16 v[70:71], v201 offset:256\n\t" \
    "s_add_u32 %4, %6, 6\n\t" \
    "s_and_b32 %4, %4, 31\n\t" \
    "s_add_u32 m0, %9, 2048\n\t" \
    "s_mul_i32 %3, %4, 57344\n\t" \
    "buffer_load_dwordx4 %7, %5, %3 offen offset:0 lds\n\t" \
    "v_mfma_f32_16x16x32_bf16 v[136:139], v[80:83], v[120:123], v[136:139]\n\t" \
    "ds_read_b128 v[96:99], v193 offset:0\n\t" \
    "ds_read_b128 v[100:103], v193 offset:16640\n\t" \
    "v_mfma_f32_16x16x32_bf16 v[140:143], v[80:83], v[124:127], v[140:143]\n\t" \
    "ds_read_b128 v[104:107], v193 offset:33280\n\t" \
    "ds_read_b128 v[108:111], v193 offset:49920\n\t" \
    "buffer_load_dwordx4 v213, %5, %3 offen offset:1024 lds\n\t" \
    "v_mfma_f32_16x16x32_bf16 v[156:159], v[84:87], v[124:127], v[156:159]\n\t" \
    "v_mfma_f32_16x16x32_bf16 v[152:155], v[84:87], v[120:123], v[152:155]\n\t" \
    "v_mfma_f32_16x16x32_bf16 v[148:151], v[84:87], v[116:119], v[148:151]\n\t" \
    "v_mfma_f32_16x16x32_bf16 v[144:147], v[84:87], v[112:115], v[144:147]\n\t" \
    "s_waitcnt vmcnt(0) lgkmcnt(0)\n\t" \
    "v_mfma_f32_16x16x32_bf16 v[128:131], v[64:67], v[96:99], v[128:131]\n\t" \
    "ds_read_b64_tr_b16 v[80:81], v200 offset:2048\n\t" \
    "ds_read_b64_tr_b16 v[82:83], v200 offset:2304\n\t" \
    "v_mfma_f32_16x16x32_bf16 v[132:135], v[64:67], v[100:103], v[132:135]\n\t" \
    "ds_read_b64_tr_b16 v[84:85], v201 offset:2048\n\t" \
    "ds_read_b64_tr_b16 v[86:87], v201 offset:2304\n\t" \
    "s_add_u32 %4, %6, 8\n\t" \
    "s_and_b32 %4, %4, 31\n\t" \
    "s_add_u32 m0, %9, 0\n\t" \
    "s_mul_i32 %3, %4, 57344\n\t" \
    "buffer_load_dwordx4 %7, %5, %3 offen offset:0 lds\n\t" \
    "v_mfma_f32_16x16x32_bf16 v[136:139], v[64:67], v[104:107], v[136:139]\n\t" \
    "ds_read_b128 v[112:115], v193 offset:64\n\t" \
    "ds_read_b128 v[116:119], v193 offset:16704\n\t" \
    "v_mfma_f32_16x16x32_bf16 v[140:143], v[64:67], v[108:111], v[140:143]\n\t" \
    "ds_read_b128 v[120:123], v193 offset:33344\n\t" \
    "ds_read_b128 v[124:127], v193 offset:49984\n\t" \
    "buffer_load_dwordx4 v213, %5, %3 offen offset:1024 lds\n\t" \
    "v_mfma_f32_16x16x32_bf16 v[156:159], v[68:71], v[108:111], v[156:159]\n\t" \
    "v_mfma_f32_16x16x32_bf16 v[152:155], v[68:71], v[104:107], v[152:155]\n\t" \
    "v_mfma_f32_16x16x32_bf16 v[148:151], v[68:71], v[100:103], v[148:151]\n\t" \
    "v_mfma_f32_16x16x32_bf16 v[144:147], v[68:71], v[96:99], v[144:147]\n\t" \
    "s_waitcnt vmcnt(0) lgkmcnt(0)\n\t" \
    "v_mfma_f32_16x16x32_bf16 v[128:131], v[80:83], v[112:115], v[128:131]\n\t" \
    "ds_read_b64_tr_b16 v[64:65], v200 offset:0\n\t" \
    "ds_read_b64_tr_b16 v[66:67], v200 offset:256\n\t" \
    "v_mfma_f32_16x16x32_bf16 v[132:135], v[80:83], v[116:119], v[132:135]\n\t" \
    "ds_read_b64_tr_b16 v[68:69], v201 offset:0\n\t" \
    "ds_read_b64_tr_b16 v[70:71], v201 offset:256\n\t" \
    "s_add_u32 %4, %6, 10\n\t" \
    "s_and_b32 %4, %4, 31\n\t" \
    "s_add_u32 m0, %9, 2048\n\t" \
    "s_mul_i32 %3, %4, 57344\n\t" \
    "buffer_load_dwordx4 %7, %5, %3 offen offset:0 lds\n\t" \
    "v_mfma_f32_16x16x32_bf16 v[136:139], v[80:83], v[120:123], v[136:139]\n\t" \
    "ds_read_b128 v[96:99], v194 offset:0\n\t" \
    "ds_read_b128 v[100:103], v194 offset:16640\n\t" \
    "v_mfma_f32_16x16x32_bf16 v[140:143], v[80:83], v[124:127], v[140:143]\n\t" \
    "ds_read_b128 v[104:107], v194 offset:33280\n\t" \
    "ds_read_b128 v[108:111], v194 offset:49920\n\t" \
    "buffer_load_dwordx4 v213, %5, %3 offen offset:1024 lds\n\t" \
    "v_mfma_f32_16x16x32_bf16 v[156:159], v[84:87], v[124:127], v[156:159]\n\t" \
    "v_mfma_f32_16x16x32_bf16 v[152:155], v[84:87], v[120:123], v[152:155]\n\t" \
    "v_mfma_f32_16x16x32_bf16 v[148:151], v[84:87], v[116:119], v[148:151]\n\t" \
    "v_mfma_f32_16x16x32_bf16 v[144:147], v[84:87], v[112:115], v[144:147]\n\t" \
    "s_waitcnt vmcnt(0) lgkmcnt(0)\n\t" \
    "v_mfma_f32_16x16x32_bf16 v[128:131], v[64:67], v[96:99], v[128:131]\n\t" \
    "ds_read_b64_tr_b16 v[80:81], v200 offset:2048\n\t" \
    "ds_read_b64_tr_b16 v[82:83], v200 offset:2304\n\t" \
    "v_mfma_f32_16x16x32_bf16 v[132:135], v[64:67], v[100:103], v[132:135]\n\t" \
    "ds_read_b64_tr_b16 v[84:85], v201 offset:2048\n\t" \
    "ds_read_b64_tr_b16 v[86:87], v201 offset:2304\n\t" \
    "s_add_u32 %4, %6, 12\n\t" \
    "s_and_b32 %4, %4, 31\n\t" \
    "s_add_u32 m0, %9, 0\n\t" \
    "s_mul_i32 %3, %4, 57344\n\t" \
    "buffer_load_dwordx4 %7, %5, %3 offen offset:0 lds\n\t" \
    "v_mfma_f32_16x16x32_bf16 v[136:139], v[64:67], v[104:107], v[136:139]\n\t" \
    "ds_read_b128 v[112:115], v194 offset:64\n\t" \
    "ds_read_b128 v[116:119], v194 offset:16704\n\t" \
    "v_mfma_f32_16x16x32_bf16 v[140:143], v[64:67], v[108:111], v[140:143]\n\t" \
    "ds_read_b128 v[120:123], v194 offset:33344\n\t" \
    "ds_read_b128 v[124:127], v194 offset:49984\n\t" \
    "buffer_load_dwordx4 v213, %5, %3 offen offset:1024 lds\n\t" \
    "v_mfma_f32_16x16x32_bf16 v[156:159], v[68:71], v[108:111], v[156:159]\n\t" \
    "v_mfma_f32_16x16x32_bf16 v[152:155], v[68:71], v[104:107], v[152:155]\n\t" \
    "v_mfma_f32_16x16x32_bf16 v[148:151], v[68:71], v[100:103], v[148:151]\n\t" \
    "v_mfma_f32_16x16x32_bf16 v[144:147], v[68:71], v[96:99], v[144:147]\n\t" \
    "s_waitcnt vmcnt(0) lgkmcnt(0)\n\t" \
    "v_mfma_f32_16x16x32_bf16 v[128:131], v[80:83], v[112:115], v[128:131]\n\t" \
    "ds_read_b64_tr_b16 v[64:65], v200 offset:0\n\t" \
    "ds_read_b64_tr_b16 v[66:67], v200 offset:256\n\t" \
    "v_mfma_f32_16x16x32_bf16 v[132:135], v[80:83], v[116:119], v[132:135]\n\t" \
    "ds_read_b64_tr_b16 v[68:69], v201 offset:0\n\t" \
    "ds_read_b64_tr_b16 v[70:71], v201 offset:256\n\t" \
    "s_add_u32 %4, %6, 14\n\t" \
    "s_and_b32 %4, %4, 31\n\t" \
    "s_add_u32 m0, %9, 2048\n\t" \
    "s_mul_i32 %3, %4, 57344\n\t" \
    "buffer_load_dwordx4 %7, %5, %3 offen offset:0 lds\n\t" \
    "v_mfma_f32_16x16x32_bf16 v[136:139], v[80:83], v[120:123], v[136:139]\n\t" \
    "ds_read_b128 v[96:99], v195 offset:0\n\t" \
    "ds_read_b128 v[100:103], v195 offset:16640\n\t" \
    "v_mfma_f32_16x16x32_bf16 v[140:143], v[80:83], v[124:127], v[140:143]\n\t" \
    "ds_read_b128 v[104:107], v195 offset:33280\n\t" \
    "ds_read_b128 v[108:111], v195 offset:49920\n\t" \
    "buffer_load_dwordx4 v213, %5, %3 offen offset:1024 lds\n\t" \
    "v_mfma_f32_16x16x32_bf16 v[156:159], v[84:87], v[124:127], v[156:159]\n\t" \
    "v_mfma_f32_16x16x32_bf16 v[152:155], v[84:87], v[120:123], v[152:155]\n\t" \
    "v_mfma_f32_16x16x32_bf16 v[148:151], v[84:87], v[116:119], v[148:151]\n\t" \
    "v_mfma_f32_16x16x32_bf16 v[144:147], v[84:87], v[112:115], v[144:147]\n\t" \
    "s_waitcnt vmcnt(0) lgkmcnt(0)\n\t" \
    "v_mfma_f32_16x16x32_bf16 v[128:131], v[64:67], v[96:99], v[128:131]\n\t" \
    "ds_read_b64_tr_b16 v[80:81], v200 offset:2048\n\t" \
    "ds_read_b64_tr_b16 v[82:83], v200 offset:2304\n\t" \
    "v_mfma_f32_16x16x32_bf16 v[132:135], v[64:67], v[100:103], v[132:135]\n\t" \
    "ds_read_b64_tr_b16 v[84:85], v201 offset:2048\n\t" \
    "ds_read_b64_tr_b16 v[86:87], v201 offset:2304\n\t" \
    "s_add_u32 %4, %6, 16\n\t" \
    "s_and_b32 %4, %4, 31\n\t" \
    "s_add_u32 m0, %9, 0\n\t" \
    "s_mul_i32 %3, %4, 57344\n\t" \
    "buffer_load_dwordx4 %7, %5, %3 offen offset:0 lds\n\t" \
    "v_mfma_f32_16x16x32_bf16 v[136:139], v[64:67], v[104:107], v[136:139]\n\t" \
    "ds_read_b128 v[112:115], v195 offset:64\n\t" \
    "ds_read_b128 v[116:119], v195 offset:16704\n\t" \
    "v_mfma_f32_16x16x32_bf16 v[140:143], v[64:67], v[108:111], v[140:143]\n\t" \
    "ds_read_b128 v[120:123], v195 offset:33344\n\t" \
    "ds_read_b128 v[124:127], v195 offset:49984\n\t" \
    "buffer_load_dwordx4 v213, %5, %3 offen offset:1024 lds\n\t" \
    "v_mfma_f32_16x16x32_bf16 v[156:159], v[68:71], v[108:111], v[156:159]\n\t" \
    "v_mfma_f32_16x16x32_bf16 v[152:155], v[68:71], v[104:107], v[152:155]\n\t" \
    "v_mfma_f32_16x16x32_bf16 v[148:151], v[68:71], v[100:103], v[148:151]\n\t" \
    "v_mfma_f32_16x16x32_bf16 v[144:147], v[68:71], v[96:99], v[144:147]\n\t" \
    "s_waitcnt vmcnt(0) lgkmcnt(0)\n\t" \
    "v_mfma_f32_16x16x32_bf16 v[128:131], v[80:83], v[112:115], v[128:131]\n\t" \
    "ds_read_b64_tr_b16 v[64:65], v200 offset:0\n\t" \
    "ds_read_b64_tr_b16 v[66:67], v200 offset:256\n\t" \
    "v_mfma_f32_16x16x32_bf16 v[132:135], v[80:83], v[116:119], v[132:135]\n\t" \
    "ds_read_b64_tr_b16 v[68:69], v201 offset:0\n\t" \
    "ds_read_b64_tr_b16 v[70:71], v201 offset:256\n\t" \
    "s_add_u32 %4, %6, 18\n\t" \
    "s_and_b32 %4, %4, 31\n\t" \
    "s_add_u32 m0, %9, 2048\n\t" \
    "s_mul_i32 %3, %4, 57344\n\t" \
    "buffer_load_dwordx4 %7, %5, %3 offen offset:0 lds\n\t" \
    "v_mfma_f32_16x16x32_bf16 v[136:139], v[80:83], v[120:123], v[136:139]\n\t" \
    "ds_read_b128 v[96:99], v196 offset:0\n\t" \
    "ds_read_b128 v[100:103], v196 offset:16640\n\t" \
    "v_mfma_f32_16x16x32_bf16 v[140:143], v[80:83], v[124:127], v[140:143]\n\t" \
    "ds_read_b128 v[104:107], v196 offset:33280\n\t" \
    "ds_read_b128 v[108:111], v196 offset:49920\n\t" \
    "buffer_load_dwordx4 v213, %5, %3 offen offset:1024 lds\n\t" \
    "v_mfma_f32_16x16x32_bf16 v[156:159], v[84:87], v[124:127], v[156:159]\n\t" \
    "v_mfma_f32_16x16x32_bf16 v[152:155], v[84:87], v[120:123], v[152:155]\n\t" \
    "v_mfma_f32_16x16x32_bf16 v[148:151], v[84:87], v[116:119], v[148:151]\n\t" \
    "v_mfma_f32_16x16x32_bf16 v[144:147], v[84:87], v[112:115], v[144:147]\n\t" \
    "s_waitcnt vmcnt(0) lgkmcnt(0)\n\t" \
    "v_mfma_f32_16x16x32_bf16 v[128:131], v[64:67], v[96:99], v[128:131]\n\t" \
    "ds_read_b64_tr_b16 v[80:81], v200 offset:2048\n\t" \
    "ds_read_b64_tr_b16 v[82:83], v200 offset:2304\n\t" \
    "v_mfma_f32_16x16x32_bf16 v[132:135], v[64:67], v[100:103], v[132:135]\n\t" \
    "ds_read_b64_tr_b16 v[84:85], v201 offset:2048\n\t" \
    "ds_read_b64_tr_b16 v[86:87], v201 offset:2304\n\t" \
    "s_add_u32 %4, %6, 20\n\t" \
    "s_and_b32 %4, %4, 31\n\t" \
    "s_add_u32 m0, %9, 0\n\t" \
    "s_mul_i32 %3, %4, 57344\n\t" \
    "buffer_load_dwordx4 %7, %5, %3 offen offset:0 lds\n\t" \
    "v_mfma_f32_16x16x32_bf16 v[136:139], v[64:67], v[104:107], v[136:139]\n\t" \
    "ds_read_b128 v[112:115], v196 offset:64\n\t" \
    "ds_read_b128 v[116:119], v196 offset:16704\n\t" \
    "v_mfma_f32_16x16x32_bf16 v[140:143], v[64:67], v[108:111], v[140:143]\n\t" \
    "ds_read_b128 v[120:123], v196 offset:33344\n\t" \
    "ds_read_b128 v[124:127], v196 offset:49984\n\t" \
    "buffer_load_dwordx4 v213, %5, %3 offen offset:1024 lds\n\t" \
    "v_mfma_f32_16x16x32_bf16 v[156:159], v[68:71], v[108:111], v[156:159]\n\t" \
    "v_mfma_f32_16x16x32_bf16 v[152:155], v[68:71], v[104:107], v[152:155]\n\t" \
    "v_mfma_f32_16x16x32_bf16 v[148:151], v[68:71], v[100:103], v[148:151]\n\t" \
    "v_mfma_f32_16x16x32_bf16 v[144:147], v[68:71], v[96:99], v[144:147]\n\t" \
    "s_waitcnt vmcnt(0) lgkmcnt(0)\n\t" \
    "v_mfma_f32_16x16x32_bf16 v[128:131], v[80:83], v[112:115], v[128:131]\n\t" \
    "ds_read_b64_tr_b16 v[64:65], v200 offset:0\n\t" \
    "ds_read_b64_tr_b16 v[66:67], v200 offset:256\n\t" \
    "v_mfma_f32_16x16x32_bf16 v[132:135], v[80:83], v[116:119], v[132:135]\n\t" \
    "ds_read_b64_tr_b16 v[68:69], v201 offset:0\n\t" \
    "ds_read_b64_tr_b16 v[70:71], v201 offset:256\n\t" \
    "s_add_u32 %4, %6, 22\n\t" \
    "s_and_b32 %4, %4, 31\n\t" \
    "s_add_u32 m0, %9, 2048\n\t" \
    "s_mul_i32 %3, %4, 57344\n\t" \
    "buffer_load_dwordx4 %7, %5, %3 offen offset:0 lds\n\t" \
    "v_mfma_f32_16x16x32_bf16 v[136:139], v[80:83], v[120:123], v[136:139]\n\t" \
    "ds_read_b128 v[96:99], v197 offset:0\n\t" \
    "ds_read_b128 v[100:103], v197 offset:16640\n\t" \
    "v_mfma_f32_16x16x32_bf16 v[140:143], v[80:83], v[124:127], v[140:143]\n\t" \
    "ds_read_b128 v[104:107], v197 offset:33280\n\t" \
    "ds_read_b128 v[108:111], v197 offset:49920\n\t" \
    "buffer_load_dwordx4 v213, %5, %3 offen offset:1024 lds\n\t" \
    "v_mfma_f32_16x16x32_bf16 v[156:159], v[84:87], v[124:127], v[156:159]\n\t" \
    "v_mfma_f32_16x16x32_bf16 v[152:155], v[84:87], v[120:123], v[152:155]\n\t" \
    "v_mfma_f32_16x16x32_bf16 v[148:151], v[84:87], v[116:119], v[148:151]\n\t" \
    "v_mfma_f32_16x16x32_bf16 v[144:147], v[84:87], v[112:115], v[144:147]\n\t" \
    "s_waitcnt vmcnt(0) lgkmcnt(0)\n\t" \
    "v_mfma_f32_16x16x32_bf16 v[128:131], v[64:67], v[96:99], v[128:131]\n\t" \
    "ds_read_b64_tr_b16 v[80:81], v200 offset:2048\n\t" \
    "ds_read_b64_tr_b16 v[82:83], v200 offset:2304\n\t" \
    "v_mfma_f32_16x16x32_bf16 v[132:135], v[64:67], v[100:103], v[132:135]\n\t" \
    "ds_read_b64_tr_b16 v[84:85], v201 offset:2048\n\t" \
    "ds_read_b64_tr_b16 v[86:87], v201 offset:2304\n\t" \
    "s_add_u32 %4, %6, 24\n\t" \
    "s_and_b32 %4, %4, 31\n\t" \
    "s_add_u32 m0, %9, 0\n\t" \
    "s_mul_i32 %3, %4, 57344\n\t" \
    "buffer_load_dwordx4 %7, %5, %3 offen offset:0 lds\n\t" \
    "v_mfma_f32_16x16x32_bf16 v[136:139], v[64:67], v[104:107], v[136:139]\n\t" \
    "ds_read_b128 v[112:115], v197 offset:64\n\t" \
    "ds_read_b128 v[116:119], v197 offset:16704\n\t" \
    "v_mfma_f32_16x16x32_bf16 v[140:143], v[64:67], v[108:111], v[140:143]\n\t" \
    "ds_read_b128 v[120:123], v197 offset:33344\n\t" \
    "ds_read_b128 v[124:127], v197 offset:49984\n\t" \
    "buffer_load_dwordx4 v213, %5, %3 offen offset:1024 lds\n\t" \
    "v_mfma_f32_16x16x32_bf16 v[156:159], v[68:71], v[108:111], v[156:159]\n\t" \
    "v_mfma_f32_16x16x32_bf16 v[152:155], v[68:71], v[104:107], v[152:155]\n\t" \
    "v_mfma_f32_16x16x32_bf16 v[148:151], v[68:71], v[100:103], v[148:151]\n\t" \
    "v_mfma_f32_16x16x32_bf16 v[144:147], v[68:71], v[96:99], v[144:147]\n\t" \
    "s_waitcnt vmcnt(0) lgkmcnt(0)\n\t" \
    "v_mfma_f32_16x16x32_bf16 v[128:131], v[80:83], v[112:115], v[128:131]\n\t" \
    "ds_read_b64_tr_b16 v[64:65], v200 offset:0\n\t" \
    "ds_read_b64_tr_b16 v[66:67], v200 offset:256\n\t" \
    "v_mfma_f32_16x16x32_bf16 v[132:135], v[80:83], v[116:119], v[132:135]\n\t" \
    "ds_read_b64_tr_b16 v[68:69], v201 offset:0\n\t" \
    "ds_read_b64_tr_b16 v[70:71], v201 offset:256\n\t" \
    "s_add_u32 %4, %6, 26\n\t" \
    "s_and_b32 %4, %4, 31\n\t" \
    "s_add_u32 m0, %9, 2048\n\t" \
    "s_mul_i32 %3, %4, 57344\n\t" \
    "buffer_load_dwordx4 %7, %5, %3 offen offset:0 lds\n\t" \
    "v_mfma_f32_16x16x32_bf16 v[136:139], v[80:83], v[120:123], v[136:139]\n\t" \
    "ds_read_b128 v[96:99], v198 offset:0\n\t" \
    "ds_read_b128 v[100:103], v198 offset:16640\n\t" \
    "v_mfma_f32_16x16x32_bf16 v[140:143], v[80:83], v[124:127], v[140:143]\n\t" \
    "ds_read_b128 v[104:107], v198 offset:33280\n\t" \
    "ds_read_b128 v[108:111], v198 offset:49920\n\t" \
    "buffer_load_dwordx4 v213, %5, %3 offen offset:1024 lds\n\t" \
    "v_mfma_f32_16x16x32_bf16 v[156:159], v[84:87], v[124:127], v[156:159]\n\t" \
    "v_mfma_f32_16x16x32_bf16 v[152:155], v[84:87], v[120:123], v[152:155]\n\t" \
    "v_mfma_f32_16x16x32_bf16 v[148:151], v[84:87], v[116:119], v[148:151]\n\t" \
    "v_mfma_f32_16x16x32_bf16 v[144:147], v[84:87], v[112:115], v[144:147]\n\t" \
    "s_waitcnt vmcnt(0) lgkmcnt(0)\n\t" \
    "v_mfma_f32_16x16x32_bf16 v[128:131], v[64:67], v[96:99], v[128:131]\n\t" \
    "ds_read_b64_tr_b16 v[80:81], v200 offset:2048\n\t" \
    "ds_read_b64_tr_b16 v[82:83], v200 offset:2304\n\t" \
    "v_mfma_f32_16x16x32_bf16 v[132:135], v[64:67], v[100:103], v[132:135]\n\t" \
    "ds_read_b64_tr_b16 v[84:85], v201 offset:2048\n\t" \
    "ds_read_b64_tr_b16 v[86:87], v201 offset:2304\n\t" \
    "s_add_u32 %4, %6, 28\n\t" \
    "s_and_b32 %4, %4, 31\n\t" \
    "s_add_u32 m0, %9, 0\n\t" \
    "s_mul_i32 %3, %4, 57344\n\t" \
    "buffer_load_dwordx4 %7, %5, %3 offen offset:0 lds\n\t" \
    "v_mfma_f32_16x16x32_bf16 v[136:139], v[64:67], v[104:107], v[136:139]\n\t" \
    "ds_read_b128 v[112:115], v198 offset:64\n\t" \
    "ds_read_b128 v[116:119], v198 offset:16704\n\t" \
    "v_mfma_f32_16x16x32_bf16 v[140:143], v[64:67], v[108:111], v[140:143]\n\t" \
    "ds_read_b128 v[120:123], v198 offset:33344\n\t" \
    "ds_read_b128 v[124:127], v198 offset:49984\n\t" \
    "buffer_load_dwordx4 v213, %5, %3 offen offset:1024 lds\n\t" \
    "v_mfma_f32_16x16x32_bf16 v[156:159], v[68:71], v[108:111], v[156:159]\n\t" \
    "v_mfma_f32_16x16x32_bf16 v[152:155], v[68:71], v[104:107], v[152:155]\n\t" \
    "v_mfma_f32_16x16x32_bf16 v[148:151], v[68:71], v[100:103], v[148:151]\n\t" \
    "v_mfma_f32_16x16x32_bf16 v[144:147], v[68:71], v[96:99], v[144:147]\n\t" \
    "s_waitcnt vmcnt(0) lgkmcnt(0)\n\t" \
    "v_mfma_f32_16x16x32_bf16 v[128:131], v[80:83], v[112:115], v[128:131]\n\t" \
    "ds_read_b64_tr_b16 v[64:65], v200 offset:0\n\t" \
    "ds_read_b64_tr_b16 v[66:67], v200 offset:256\n\t" \
    "v_mfma_f32_16x16x32_bf16 v[132:135], v[80:83], v[116:119], v[132:135]\n\t" \
    "ds_read_b64_tr_b16 v[68:69], v201 offset:0\n\t" \
    "ds_read_b64_tr_b16 v[70:71], v201 offset:256\n\t" \
    "s_add_u32 %4, %6, 30\n\t" \
    "s_and_b32 %4, %4, 31\n\t" \
    "s_add_u32 m0, %9, 2048\n\t" \
    "s_mul_i32 %3, %4, 57344\n\t" \
    "buffer_load_dwordx4 %7, %5, %3 offen offset:0 lds\n\t" \
    "v_mfma_f32_16x16x32_bf16 v[136:139], v[80:83], v[120:123], v[136:139]\n\t" \
    "ds_read_b128 v[96:99], v199 offset:0\n\t" \
    "ds_read_b128 v[100:103], v199 offset:16640\n\t" \
    "v_mfma_f32_16x16x32_bf16 v[140:143], v[80:83], v[124:127], v[140:143]\n\t" \
    "ds_read_b128 v[104:107], v199 offset:33280\n\t" \
    "ds_read_b128 v[108:111], v199 offset:49920\n\t" \
    "buffer_load_dwordx4 v213, %5, %3 offen offset:1024 lds\n\t" \
    "v_mfma_f32_16x16x32_bf16 v[156:159], v[84:87], v[124:127], v[156:159]\n\t" \
    "v_mfma_f32_16x16x32_bf16 v[152:155], v[84:87], v[120:123], v[152:155]\n\t" \
    "v_mfma_f32_16x16x32_bf16 v[148:151], v[84:87], v[116:119], v[148:151]\n\t" \
    "v_mfma_f32_16x16x32_bf16 v[144:147], v[84:87], v[112:115], v[144:147]\n\t" \
    "s_waitcnt vmcnt(0) lgkmcnt(0)\n\t" \
    "v_mfma_f32_16x16x32_bf16 v[128:131], v[64:67], v[96:99], v[128:131]\n\t" \
    "ds_read_b64_tr_b16 v[80:81], v200 offset:2048\n\t" \
    "ds_read_b64_tr_b16 v[82:83], v200 offset:2304\n\t" \
    "v_mfma_f32_16x16x32_bf16 v[132:135], v[64:67], v[100:103], v[132:135]\n\t" \
    "ds_read_b64_tr_b16 v[84:85], v201 offset:2048\n\t" \
    "ds_read_b64_tr_b16 v[86:87], v201 offset:2304\n\t" \
    "v_mfma_f32_16x16x32_bf16 v[136:139], v[64:67], v[104:107], v[136:139]\n\t" \
    "ds_read_b128 v[112:115], v199 offset:64\n\t" \
    "ds_read_b128 v[116:119], v199 offset:16704\n\t" \
    "v_mfma_f32_16x16x32_bf16 v[140:143], v[64:67], v[108:111], v[140:143]\n\t" \
    "ds_read_b128 v[120:123], v199 offset:33344\n\t" \
    "ds_read_b128 v[124:127], v199 offset:49984\n\t" \
    "v_mfma_f32_16x16x32_bf16 v[156:159], v[68:71], v[108:111], v[156:159]\n\t" \
    "v_mfma_f32_16x16x32_bf16 v[152:155], v[68:71], v[104:107], v[152:155]\n\t" \
    "v_mfma_f32_16x16x32_bf16 v[148:151], v[68:71], v[100:103], v[148:151]\n\t" \
    "v_mfma_f32_16x16x32_bf16 v[144:147], v[68:71], v[96:99], v[144:147]\n\t" \
    "s_waitcnt lgkmcnt(0)\n\t" \
    "v_mfma_f32_16x16x32_bf16 v[128:131], v[80:83], v[112:115], v[128:131]\n\t" \
    "v_mfma_f32_16x16x32_bf16 v[132:135], v[80:83], v[116:119], v[132:135]\n\t" \
    "v_mfma_f32_16x16x32_bf16 v[136:139], v[80:83], v[120:123], v[136:139]\n\t" \
    "v_mfma_f32_16x16x32_bf16 v[140:143], v[80:83], v[124:127], v[140:143]\n\t" \
    "v_mfma_f32_16x16x32_bf16 v[156:159], v[84:87], v[124:127], v[156:159]\n\t" \
    "v_mfma_f32_16x16x32_bf16 v[152:155], v[84:87], v[120:123], v[152:155]\n\t" \
    "v_mfma_f32_16x16x32_bf16 v[148:151], v[84:87], v[116:119], v[148:151]\n\t" \
    "v_mfma_f32_16x16x32_bf16 v[144:147], v[84:87], v[112:115], v[144:147]\n\t" \
    "s_setprio 2\n\t" \
    "s_add_u32 %3, %13, 24\n\t" \
    "s_mov_b32 %4, 0\n\t" \
    "9:\n\t" \
    "v_mov_b32 v208, %3\n\t" \
    "ds_read_b32 v208, v208\n\t" \
    "s_waitcnt lgkmcnt(0)\n\t" \
    "v_readfirstlane_b32 vcc_lo, v208\n\t" \
    "s_add_u32 %4, %4, 1\n\t" \
    "s_cmp_ge_u32 vcc_lo, 4\n\t" \
    "s_cbranch_scc1 90f\n\t" \
    "s_sleep 1\n\t" \
    "s_cmp_lt_u32 %4, 0x400000\n\t" \
    "s_cbranch_scc1 9b\n\t" \
    "90:\n\t" \
    "v_add_u32 v213, 7168, %15\n\t" \
    "v_add_u32 v217, 7168, %23\n\t" \
    "v_add_u32 v214, 14336, %15\n\t" \
    "v_add_u32 v218, 14336, %23\n\t" \
    "v_add_u32 v215, 21504, %15\n\t" \
    "v_add_u32 v219, 21504, %23\n\t" \
    "s_add_u32 %3, %16, 0\n\t" \
    "s_add_u32 m0, %9, 0\n\t" \
    "s_lshl_b32 %3, %3, 15\n\t" \
    "buffer_load_dwordx4 %15, %14, %3 offen offset:0 lds\n\t" \
    "buffer_load_dwordx4 v213, %14, %3 offen offset:1024 lds\n\t" \
    "buffer_load_dwordx4 v214, %14, %3 offen offset:2048 lds\n\t" \
    "buffer_load_dwordx4 v215, %14, %3 offen offset:3072 lds\n\t" \
    "s_add_u32 m0, %10, 0\n\t" \
    "s_lshl_b32 %3, %21, 17\n\t" \
    "s_add_u32 %3, %3, 0\n\t" \
    "buffer_load_dwordx4 %23, %22, %3 offen offset:0 lds\n\t" \
    "buffer_load_dwordx4 v217, %22, %3 offen offset:1024 lds\n\t" \
    "buffer_load_dwordx4 v218, %22, %3 offen offset:2048 lds\n\t" \
    "buffer_load_dwordx4 v219, %22, %3 offen offset:3072 lds\n\t" \
    "s_add_u32 m0, %10, 4096\n\t" \
    "s_lshl_b32 %3, %21, 17\n\t" \
    "s_add_u32 %3, %3, 32768\n\t" \
    "buffer_load_dwordx4 %23, %22, %3 offen offset:0 lds\n\t" \
    "buffer_load_dwordx4 v217, %22, %3 offen offset:1024 lds\n\t" \
    "buffer_load_dwordx4 v218, %22, %3 offen offset:2048 lds\n\t" \
    "buffer_load_dwordx4 v219, %22, %3 offen offset:3072 lds\n\t" \
    "s_add_u32 %4, %21, 0\n\t" \
    "s_and_b32 %4, %4, 3\n\t" \
    "s_lshl_b32 %4, %4, 5\n\t" \
    "v_xor_b32 v224, %4, %20\n\t" \
    "v_add_u32 v224, %9, v224\n\t" \
    "v_add_u32 v244, %4, %24\n\t" \
    "s_add_u32 %4, %21, 1\n\t" \
    "s_and_b32 %4, %4, 3\n\t" \
    "s_lshl_b32 %4, %4, 5\n\t" \
    "v_xor_b32 v225, %4, %20\n\t" \
    "v_add_u32 v225, %9, v225\n\t" \
    "v_add_u32 v245, %4, %24\n\t" \
    "s_add_u32 %4, %21, 2\n\t" \
    "s_and_b32 %4, %4, 3\n\t" \
    "s_lshl_b32 %4, %4, 5\n\t" \
    "v_xor_b32 v226, %4, %20\n\t" \
    "v_add_u32 v226, %9, v226\n\t" \
    "v_add_u32 v246, %4, %24\n\t" \
    "s_add_u32 %4, %21, 3\n\t" \
    "s_and_b32 %4, %4, 3\n\t" \
    "s_lshl_b32 %4, %4, 5\n\t" \
    "v_xor_b32 v227, %4, %20\n\t" \
    "v_add_u32 v227, %9, v227\n\t" \
    "v_add_u32 v247, %4, %24\n\t" \
    "v_cvt_pk_bf16_f32 v192, v128, v129\n\t" \
    "v_cvt_pk_bf16_f32 v193, v130, v131\n\t" \
    "v_cvt_pk_bf16_f32 v194, v144, v145\n\t" \
    "v_cvt_pk_bf16_f32 v195, v146, v147\n\t" \
    "v_pk_max_i16 v192, v192, 0\n\t" \
    "v_pk_max_i16 v193, v193, 0\n\t" \
    "v_pk_max_i16 v194, v194, 0\n\t" \
    "v_pk_max_i16 v195, v195, 0\n\t" \
    "buffer_store_dwordx4 v[192:195], %18, %17, %19 offen offset:0 sc1\n\t" \
    "v_cvt_pk_bf16_f32 v196, v132, v133\n\t" \
    "v_cvt_pk_bf16_f32 v197, v134, v135\n\t" \
    "v_cvt_pk_bf16_f32 v198, v148, v149\n\t" \
    "v_cvt_pk_bf16_f32 v199, v150, v151\n\t" \
    "v_pk_max_i16 v196, v196, 0\n\t" \
    "v_pk_max_i16 v197, v197, 0\n\t" \
    "v_pk_max_i16 v198, v198, 0\n\t" \
    "v_pk_max_i16 v199, v199, 0\n\t" \
    "buffer_store_dwordx4 v[196:199], %18, %17, %19 offen offset:1024 sc1\n\t" \
    "v_cvt_pk_bf16_f32 v200, v136, v137\n\t" \
    "v_cvt_pk_bf16_f32 v201, v138, v139\n\t" \
    "v_cvt_pk_bf16_f32 v202, v152, v153\n\t" \
    "v_cvt_pk_bf16_f32 v203, v154, v155\n\t" \
    "v_pk_max_i16 v200, v200, 0\n\t" \
    "v_pk_max_i16 v201, v201, 0\n\t" \
    "v_pk_max_i16 v202, v202, 0\n\t" \
    "v_pk_max_i16 v203, v203, 0\n\t" \
    "buffer_store_dwordx4 v[200:203], %18, %17, %19 offen offset:2048 sc1\n\t" \
    "v_cvt_pk_bf16_f32 v204, v140, v141\n\t" \
    "v_cvt_pk_bf16_f32 v205, v142, v143\n\t" \
    "v_cvt_pk_bf16_f32 v206, v156, v157\n\t" \
    "v_cvt_pk_bf16_f32 v207, v158, v159\n\t" \
    "v_pk_max_i16 v204, v204, 0\n\t" \
    "v_pk_max_i16 v205, v205, 0\n\t" \
    "v_pk_max_i16 v206, v206, 0\n\t" \
    "v_pk_max_i16 v207, v207, 0\n\t" \
    "buffer_store_dwordx4 v[204:207], %18, %17, %19 offen offset:3072 sc1\n\t" \
    "s_waitcnt vmcnt(4)\n\t" \
    "s_setprio 2\n\t" \
    "ds_read_b64_tr_b16 v[32:33], v244 offset:0\n\t" \
    "ds_read_b64_tr_b16 v[34:35], v244 offset:1024\n\t" \
    "ds_read_b64_tr_b16 v[36:37], v245 offset:0\n\t" \
    "ds_read_b64_tr_b16 v[38:39], v245 offset:1024\n\t" \
    "ds_read_b64_tr_b16 v[40:41], v246 offset:0\n\t" \
    "ds_read_b64_tr_b16 v[42:43], v246 offset:1024\n\t" \
    "ds_read_b64_tr_b16 v[44:45], v247 offset:0\n\t" \
    "ds_read_b64_tr_b16 v[46:47], v247 offset:1024\n\t" \
    "ds_read_b64_tr_b16 v[48:49], v244 offset:4096\n\t" \
    "ds_read_b64_tr_b16 v[50:51], v244 offset:5120\n\t" \
    "ds_read_b64_tr_b16 v[52:53], v245 offset:4096\n\t" \
    "ds_read_b64_tr_b16 v[54:55], v245 offset:5120\n\t" \
    "ds_read_b64_tr_b16 v[56:57], v246 offset:4096\n\t" \
    "ds_read_b64_tr_b16 v[58:59], v246 offset:5120\n\t" \
    "ds_read_b64_tr_b16 v[60:61], v247 offset:4096\n\t" \
    "ds_read_b64_tr_b16 v[62:63], v247 offset:5120\n\t" \
    "ds_read_b64_tr_b16 v[228:229], v224 offset:0\n\t" \
    "ds_read_b64_tr_b16 v[230:231], v224 offset:2048\n\t" \
    "ds_read_b64_tr_b16 v[232:233], v225 offset:0\n\t" \
    "ds_read_b64_tr_b16 v[234:235], v225 offset:2048\n\t" \
    "ds_read_b64_tr_b16 v[236:237], v226 offset:0\n\t" \
    "ds_read_b64_tr_b16 v[238:239], v226 offset:2048\n\t" \
    "ds_read_b64_tr_b16 v[240:241], v227 offset:0\n\t" \
    "ds_read_b64_tr_b16 v[242:243], v227 offset:2048\n\t" \
    "s_waitcnt lgkmcnt(6)\n\t" \
    "v_mfma_f32_16x16x32_bf16 v[64:67], v[228:231], v[192:195], 0\n\t" \
    "v_mfma_f32_16x16x32_bf16 v[68:71], v[228:231], v[196:199], 0\n\t" \
    "v_mfma_f32_16x16x32_bf16 v[72:75], v[228:231], v[200:203], 0\n\t" \
    "v_mfma_f32_16x16x32_bf16 v[76:79], v[228:231], v[204:207], 0\n\t" \
    "s_waitcnt lgkmcnt(4)\n\t" \
    "v_mfma_f32_16x16x32_bf16 v[92:95], v[232:235], v[204:207], 0\n\t" \
    "v_mfma_f32_16x16x32_bf16 v[88:91], v[232:235], v[200:203], 0\n\t" \
    "v_mfma_f32_16x16x32_bf16 v[84:87], v[232:235], v[196:199], 0\n\t" \
    "v_mfma_f32_16x16x32_bf16 v[80:83], v[232:235], v[192:195], 0\n\t" \
    "s_waitcnt lgkmcnt(2)\n\t" \
    "v_mfma_f32_16x16x32_bf16 v[96:99], v[236:239], v[192:195], 0\n\t" \
    "v_mfma_f32_16x16x32_bf16 v[100:103], v[236:239], v[196:199], 0\n\t" \
    "v_mfma_f32_16x16x32_bf16 v[104:107], v[236:239], v[200:203], 0\n\t" \
    "v_mfma_f32_16x16x32_bf16 v[108:111], v[236:239], v[204:207], 0\n\t" \
    "s_waitcnt lgkmcnt(0)\n\t" \
    "v_mfma_f32_16x16x32_bf16 v[124:127], v[240:243], v[204:207], 0\n\t" \
    "v_mfma_f32_16x16x32_bf16 v[120:123], v[240:243], v[200:203], 0\n\t" \
    "v_mfma_f32_16x16x32_bf16 v[116:119], v[240:243], v[196:199], 0\n\t" \
    "v_mfma_f32_16x16x32_bf16 v[112:115], v[240:243], v[192:195], 0\n\t" \
    "s_setprio 2\n\t" \
    "s_nop 7\n\t" \
    "92:\n\t"
#define CLOB_UP "v128","v129","v130","v131","v132","v133","v134","v135","v136","v137","v138","v139","v140","v141","v142","v143","v144","v145","v146","v147","v148","v149","v150","v151","v152","v153","v154","v155","v156","v157","v158","v159","v160","v161","v162","v163","v164","v165","v166","v167","v168","v169","v170","v171","v172","v173","v174","v175","v176","v177","v178","v179","v180","v181","v182","v183","v184","v185","v186","v187","v188","v189","v190","v191","v192","v193","v194","v195","v196","v197","v198","v199","v200","v201","v202","v203","v204","v205","v206","v207","v208","v209","v210","v211","v212","v213","v214","v215","v216","v217","v218","v219","v220","v221","v222","v223","v224","v225","v226","v227","v228","v229","v230","v231","v232","v233","v234","v235","v236","v237","v238","v239","v240","v241","v242","v243","v244","v245","v246","v247"
#define ASM_DOWN \
    "s_cmp_eq_u32 %15, 3\n\t" \
    "s_cbranch_scc1 91f\n\t" \
    "s_add_u32 %4, %15, 0\n\t" \
    "s_and_b32 %4, %4, 3\n\t" \
    "s_lshl_b32 %4, %4, 5\n\t" \
    "v_xor_b32 v240, %4, %14\n\t" \
    "v_add_u32 v244, %4, %22\n\t" \
    "v_add_u32 v240, %11, v240\n\t" \
    "s_add_u32 %4, %15, 1\n\t" \
    "s_and_b32 %4, %4, 3\n\t" \
    "s_lshl_b32 %4, %4, 5\n\t" \
    "v_xor_b32 v241, %4, %14\n\t" \
    "v_add_u32 v245, %4, %22\n\t" \
    "v_add_u32 v241, %11, v241\n\t" \
    "s_add_u32 %4, %15, 2\n\t" \
    "s_and_b32 %4, %4, 3\n\t" \
    "s_lshl_b32 %4, %4, 5\n\t" \
    "v_xor_b32 v242, %4, %14\n\t" \
    "v_add_u32 v246, %4, %22\n\t" \
    "v_add_u32 v242, %11, v242\n\t" \
    "s_add_u32 %4, %15, 3\n\t" \
    "s_and_b32 %4, %4, 3\n\t" \
    "s_lshl_b32 %4, %4, 5\n\t" \
    "v_xor_b32 v243, %4, %14\n\t" \
    "v_add_u32 v247, %4, %22\n\t" \
    "v_add_u32 v243, %11, v243\n\t" \
    "v_add_u32 v249, 7168, %12\n\t" \
    "v_add_u32 v250, 14336, %12\n\t" \
    "v_add_u32 v251, 21504, %12\n\t" \
    "s_mov_b32 vcc_hi, %3\n\t" \
    "s_add_u32 %4, %9, %3\n\t" \
    "s_cmp_ge_u32 %4, %23\n\t" \
    "s_cselect_b32 %5, 7, 0\n\t" \
    "s_add_u32 %4, %4, %5\n\t" \
    "s_lshl_b32 %5, %4, 12\n\t" \
    "s_add_u32 m0, %10, 0\n\t" \
    "s_lshl_b32 %4, %4, 15\n\t" \
    "buffer_load_dwordx4 %12, %7, %4 offen offset:0 lds\n\t" \
    "buffer_load_dwordx4 v249, %7, %4 offen offset:1024 lds\n\t" \
    "buffer_load_dwordx4 v250, %7, %4 offen offset:2048 lds\n\t" \
    "buffer_load_dwordx4 v251, %7, %4 offen offset:3072 lds\n\t" \
    "s_add_u32 %3, %3, 1\n\t" \
    "s_cmp_eq_u32 %3, 12\n\t" \
    "s_cselect_b32 %3, 0, %3\n\t" \
    "s_add_u32 %4, %9, %3\n\t" \
    "s_cmp_ge_u32 %4, %23\n\t" \
    "s_cselect_b32 %5, 7, 0\n\t" \
    "s_add_u32 %4, %4, %5\n\t" \
    "s_lshl_b32 %5, %4, 12\n\t" \
    "s_add_u32 m0, %10, 4096\n\t" \
    "s_lshl_b32 %4, %4, 15\n\t" \
    "buffer_load_dwordx4 %12, %7, %4 offen offset:0 lds\n\t" \
    "buffer_load_dwordx4 v249, %7, %4 offen offset:1024 lds\n\t" \
    "buffer_load_dwordx4 v250, %7, %4 offen offset:2048 lds\n\t" \
    "buffer_load_dwordx4 v251, %7, %4 offen offset:3072 lds\n\t" \
    "s_add_u32 %3, %3, 1\n\t" \
    "s_cmp_eq_u32 %3, 12\n\t" \
    "s_cselect_b32 %3, 0, %3\n\t" \
    "s_add_u32 %4, %9, %3\n\t" \
    "s_cmp_ge_u32 %4, %23\n\t" \
    "s_cselect_b32 %5, 7, 0\n\t" \
    "s_add_u32 %4, %4, %5\n\t" \
    "s_lshl_b32 %5, %4, 12\n\t" \
    "s_add_u32 m0, %11, 0\n\t" \
    "s_lshl_b32 %4, %4, 15\n\t" \
    "buffer_load_dwordx4 %12, %7, %4 offen offset:0 lds\n\t" \
    "buffer_load_dwordx4 v249, %7, %4 offen offset:1024 lds\n\t" \
    "buffer_load_dwordx4 v250, %7, %4 offen offset:2048 lds\n\t" \
    "buffer_load_dwordx4 v251, %7, %4 offen offset:3072 lds\n\t" \
    "s_add_u32 %3, %3, 1\n\t" \
    "s_cmp_eq_u32 %3, 12\n\t" \
    "s_cselect_b32 %3, 0, %3\n\t" \
    "s_mov_b32 %6, 0\n\t" \
    "1:\n\t" \
    "buffer_load_dwordx2 v[236:237], %17, %16, 0 offen sc1\n\t" \
    "s_waitcnt vmcnt(0)\n\t" \
    "v_cmp_eq_u64 s[64:65], %18, v[236:237]\n\t" \
    "s_add_u32 %6, %6, 1\n\t" \
    "s_cmp_eq_u64 s[64:65], -1\n\t" \
    "s_cbranch_scc1 10f\n\t" \
    "s_sleep 4\n\t" \
    "s_cmp_lt_u32 %6, 0x400000\n\t" \
    "s_cbranch_scc1 1b\n\t" \
    "10:\n\t" \
    "s_mov_b32 %3, vcc_hi\n\t" \
    "s_add_u32 %4, %9, %3\n\t" \
    "s_cmp_ge_u32 %4, %23\n\t" \
    "s_cselect_b32 %5, 7, 0\n\t" \
    "s_add_u32 %4, %4, %5\n\t" \
    "s_lshl_b32 %5, %4, 12\n\t" \
    "buffer_load_dwordx4 v[128:131], %13, %8, %5 offen offset:0 sc1\n\t" \
    "buffer_load_dwordx4 v[132:135], %13, %8, %5 offen offset:1024 sc1\n\t" \
    "buffer_load_dwordx4 v[136:139], %13, %8, %5 offen offset:2048 sc1\n\t" \
    "buffer_load_dwordx4 v[140:143], %13, %8, %5 offen offset:3072 sc1\n\t" \
    "s_add_u32 %3, %3, 1\n\t" \
    "s_cmp_eq_u32 %3, 12\n\t" \
    "s_cselect_b32 %3, 0, %3\n\t" \
    "s_add_u32 %4, %9, %3\n\t" \
    "s_cmp_ge_u32 %4, %23\n\t" \
    "s_cselect_b32 %5, 7, 0\n\t" \
    "s_add_u32 %4, %4, %5\n\t" \
    "s_lshl_b32 %5, %4, 12\n\t" \
    "buffer_load_dwordx4 v[144:147], %13, %8, %5 offen offset:0 sc1\n\t" \
    "buffer_load_dwordx4 v[148:151], %13, %8, %5 offen offset:1024 sc1\n\t" \
    "buffer_load_dwordx4 v[152:155], %13, %8, %5 offen offset:2048 sc1\n\t" \
    "buffer_load_dwordx4 v[156:159], %13, %8, %5 offen offset:3072 sc1\n\t" \
    "s_add_u32 %3, %3, 1\n\t" \
    "s_cmp_eq_u32 %3, 12\n\t" \
    "s_cselect_b32 %3, 0, %3\n\t" \
    "s_add_u32 %4, %9, %3\n\t" \
    "s_cmp_ge_u32 %4, %23\n\t" \
    "s_cselect_b32 %5, 7, 0\n\t" \
    "s_add_u32 %4, %4, %5\n\t" \
    "s_lshl_b32 %5, %4, 12\n\t" \
    "buffer_load_dwordx4 v[160:163], %13, %8, %5 offen offset:0 sc1\n\t" \
    "buffer_load_dwordx4 v[164:167], %13, %8, %5 offen offset:1024 sc1\n\t" \
    "buffer_load_dwordx4 v[168:171], %13, %8, %5 offen offset:2048 sc1\n\t" \
    "buffer_load_dwordx4 v[172:175], %13, %8, %5 offen offset:3072 sc1\n\t" \
    "s_add_u32 %3, %3, 1\n\t" \
    "s_cmp_eq_u32 %3, 12\n\t" \
    "s_cselect_b32 %3, 0, %3\n\t" \
    "s_setprio 2\n\t" \
    "ds_read_b64_tr_b16 v[192:193], v240 offset:38912\n\t" \
    "ds_read_b64_tr_b16 v[194:195], v240 offset:40960\n\t" \
    "ds_read_b64_tr_b16 v[196:197], v241 offset:38912\n\t" \
    "ds_read_b64_tr_b16 v[198:199], v241 offset:40960\n\t" \
    "ds_read_b64_tr_b16 v[200:201], v242 offset:38912\n\t" \
    "ds_read_b64_tr_b16 v[202:203], v242 offset:40960\n\t" \
    "ds_read_b64_tr_b16 v[204:205], v243 offset:38912\n\t" \
    "ds_read_b64_tr_b16 v[206:207], v243 offset:40960\n\t" \
    "s_waitcnt vmcnt(8)\n\t" \
    "s_waitcnt lgkmcnt(6)\n\t" \
    "v_mfma_f32_16x16x32_bf16 v[64:67], v[192:195], v[128:131], v[64:67]\n\t" \
    "s_add_u32 %4, %9, %3\n\t" \
    "s_cmp_ge_u32 %4, %23\n\t" \
    "s_cselect_b32 %5, 7, 0\n\t" \
    "s_add_u32 %4, %4, %5\n\t" \
    "s_lshl_b32 %5, %4, 12\n\t" \
    "s_add_u32 m0, %11, 4096\n\t" \
    "s_lshl_b32 %4, %4, 15\n\t" \
    "buffer_load_dwordx4 %12, %7, %4 offen offset:0 lds\n\t" \
    "v_mfma_f32_16x16x32_bf16 v[68:71], v[192:195], v[132:135], v[68:71]\n\t" \
    "v_mfma_f32_16x16x32_bf16 v[72:75], v[192:195], v[136:139], v[72:75]\n\t" \
    "buffer_load_dwordx4 v249, %7, %4 offen offset:1024 lds\n\t" \
    "v_mfma_f32_16x16x32_bf16 v[76:79], v[192:195], v[140:143], v[76:79]\n\t" \
    "ds_read_b64_tr_b16 v[192:193], v240 offset:43008\n\t" \
    "ds_read_b64_tr_b16 v[194:195], v240 offset:45056\n\t" \
    "s_waitcnt lgkmcnt(6)\n\t" \
    "v_mfma_f32_16x16x32_bf16 v[92:95], v[196:199], v[140:143], v[92:95]\n\t" \
    "buffer_load_dwordx4 v250, %7, %4 offen offset:2048 lds\n\t" \
    "v_mfma_f32_16x16x32_bf16 v[88:91], v[196:199], v[136:139], v[88:91]\n\t" \
    "v_mfma_f32_16x16x32_bf16 v[84:87], v[196:199], v[132:135], v[84:87]\n\t" \
    "buffer_load_dwordx4 v251, %7, %4 offen offset:3072 lds\n\t" \
    "v_mfma_f32_16x16x32_bf16 v[80:83], v[196:199], v[128:131], v[80:83]\n\t" \
    "ds_read_b64_tr_b16 v[196:197], v241 offset:43008\n\t" \
    "ds_read_b64_tr_b16 v[198:199], v241 offset:45056\n\t" \
    "s_waitcnt lgkmcnt(6)\n\t" \
    "v_mfma_f32_16x16x32_bf16 v[96:99], v[200:203], v[128:131], v[96:99]\n\t" \
    "buffer_load_dwordx4 v[176:179], %13, %8, %5 offen offset:0 sc1\n\t" \
    "v_mfma_f32_16x16x32_bf16 v[100:103], v[200:203], v[132:135], v[100:103]\n\t" \
    "v_mfma_f32_16x16x32_bf16 v[104:107], v[200:203], v[136:139], v[104:107]\n\t" \
    "buffer_load_dwordx4 v[180:183], %13, %8, %5 offen offset:1024 sc1\n\t" \
    "v_mfma_f32_16x16x32_bf16 v[108:111], v[200:203], v[140:143], v[108:111]\n\t" \
    "ds_read_b64_tr_b16 v[200:201], v242 offset:43008\n\t" \
    "ds_read_b64_tr_b16 v[202:203], v242 offset:45056\n\t" \
    "s_waitcnt lgkmcnt(6)\n\t" \
    "v_mfma_f32_16x16x32_bf16 v[124:127], v[204:207], v[140:143], v[124:127]\n\t" \
    "buffer_load_dwordx4 v[184:187], %13, %8, %5 offen offset:2048 sc1\n\t" \
    "v_mfma_f32_16x16x32_bf16 v[120:123], v[204:207], v[136:139], v[120:123]\n\t" \
    "v_mfma_f32_16x16x32_bf16 v[116:119], v[204:207], v[132:135], v[116:119]\n\t" \
    "buffer_load_dwordx4 v[188:191], %13, %8, %5 offen offset:3072 sc1\n\t" \
    "s_add_u32 %3, %3, 1\n\t" \
    "s_cmp_eq_u32 %3, 12\n\t" \
    "s_cselect_b32 %3, 0, %3\n\t" \
    "v_mfma_f32_16x16x32_bf16 v[112:115], v[204:207], v[128:131], v[112:115]\n\t" \
    "ds_read_b64_tr_b16 v[204:205], v243 offset:43008\n\t" \
    "ds_read_b64_tr_b16 v[206:207], v243 offset:45056\n\t" \
    "s_waitcnt vmcnt(12)\n\t" \
    "s_waitcnt lgkmcnt(6)\n\t" \
    "v_mfma_f32_16x16x32_bf16 v[64:67], v[192:195], v[144:147], v[64:67]\n\t" \
    "s_add_u32 %4, %9, %3\n\t" \
    "s_cmp_ge_u32 %4, %23\n\t" \
    "s_cselect_b32 %5, 7, 0\n\t" \
    "s_add_u32 %4, %4, %5\n\t" \
    "s_lshl_b32 %5, %4, 12\n\t" \
    "s_add_u32 m0, %10, 0\n\t" \
    "s_lshl_b32 %4, %4, 15\n\t" \
    "buffer_load_dwordx4 %12, %7, %4 offen offset:0 lds\n\t" \
    "v_mfma_f32_16x16x32_bf16 v[68:71], v[192:195], v[148:151], v[68:71]\n\t" \
    "v_mfma_f32_16x16x32_bf16 v[72:75], v[192:195], v[152:155], v[72:75]\n\t" \
    "buffer_load_dwordx4 v249, %7, %4 offen offset:1024 lds\n\t" \
    "v_mfma_f32_16x16x32_bf16 v[76:79], v[192:195], v[156:159], v[76:79]\n\t" \
    "ds_read_b64_tr_b16 v[192:193], v240 offset:0\n\t" \
    "ds_read_b64_tr_b16 v[194:195], v240 offset:2048\n\t" \
    "s_waitcnt lgkmcnt(6)\n\t" \
    "v_mfma_f32_16x16x32_bf16 v[92:95], v[196:199], v[156:159], v[92:95]\n\t" \
    "buffer_load_dwordx4 v250, %7, %4 offen offset:2048 lds\n\t" \
    "v_mfma_f32_16x16x32_bf16 v[88:91], v[196:199], v[152:155], v[88:91]\n\t" \
    "v_mfma_f32_16x16x32_bf16 v[84:87], v[196:199], v[148:151], v[84:87]\n\t" \
    "buffer_load_dwordx4 v251, %7, %4 offen offset:3072 lds\n\t" \
    "v_mfma_f32_16x16x32_bf16 v[80:83], v[196:199], v[144:147], v[80:83]\n\t" \
    "ds_read_b64_tr_b16 v[196:197], v241 offset:0\n\t" \
    "ds_read_b64_tr_b16 v[198:199], v241 offset:2048\n\t" \
    "s_waitcnt lgkmcnt(6)\n\t" \
    "v_mfma_f32_16x16x32_bf16 v[96:99], v[200:203], v[144:147], v[96:99]\n\t" \
    "buffer_load_dwordx4 v[128:131], %13, %8, %5 offen offset:0 sc1\n\t" \
    "v_mfma_f32_16x16x32_bf16 v[100:103], v[200:203], v[148:151], v[100:103]\n\t" \
    "v_mfma_f32_16x16x32_bf16 v[104:107], v[200:203], v[152:155], v[104:107]\n\t" \
    "buffer_load_dwordx4 v[132:135], %13, %8, %5 offen offset:1024 sc1\n\t" \
    "v_mfma_f32_16x16x32_bf16 v[108:111], v[200:203], v[156:159], v[108:111]\n\t" \
    "ds_read_b64_tr_b16 v[200:201], v242 offset:0\n\t" \
    "ds_read_b64_tr_b16 v[202:203], v242 offset:2048\n\t" \
    "s_waitcnt lgkmcnt(6)\n\t" \
    "v_mfma_f32_16x16x32_bf16 v[124:127], v[204:207], v[156:159], v[124:127]\n\t" \
    "buffer_load_dwordx4 v[136:139], %13, %8, %5 offen offset:2048 sc1\n\t" \
    "v_mfma_f32_16x16x32_bf16 v[120:123], v[204:207], v[152:155], v[120:123]\n\t" \
    "v_mfma_f32_16x16x32_bf16 v[116:119], v[204:207], v[148:151], v[116:119]\n\t" \
    "buffer_load_dwordx4 v[140:143], %13, %8, %5 offen offset:3072 sc1\n\t" \
    "s_add_u32 %3, %3, 1\n\t" \
    "s_cmp_eq_u32 %3, 12\n\t" \
    "s_cselect_b32 %3, 0, %3\n\t" \
    "v_mfma_f32_16x16x32_bf16 v[112:115], v[204:207], v[144:147], v[112:115]\n\t" \
    "ds_read_b64_tr_b16 v[204:205], v243 offset:0\n\t" \
    "ds_read_b64_tr_b16 v[206:207], v243 offset:2048\n\t" \
    "s_waitcnt vmcnt(12)\n\t" \
    "s_waitcnt lgkmcnt(6)\n\t" \
    "v_mfma_f32_16x16x32_bf16 v[64:67], v[192:195], v[160:163], v[64:67]\n\t" \
    "s_add_u32 %4, %9, %3\n\t" \
    "s_cmp_ge_u32 %4, %23\n\t" \
    "s_cselect_b32 %5, 7, 0\n\t" \
    "s_add_u32 %4, %4, %5\n\t" \
    "s_lshl_b32 %5, %4, 12\n\t" \
    "s_add_u32 m0, %10, 4096\n\t" \
    "s_lshl_b32 %4, %4, 15\n\t" \
    "buffer_load_dwordx4 %12, %7, %4 offen offset:0 lds\n\t" \
    "v_mfma_f32_16x16x32_bf16 v[68:71], v[192:195], v[164:167], v[68:71]\n\t" \
    "v_mfma_f32_16x16x32_bf16 v[72:75], v[192:195], v[168:171], v[72:75]\n\t" \
    "buffer_load_dwordx4 v249, %7, %4 offen offset:1024 lds\n\t" \
    "v_mfma_f32_16x16x32_bf16 v[76:79], v[192:195], v[172:175], v[76:79]\n\t" \
    "ds_read_b64_tr_b16 v[192:193], v240 offset:4096\n\t" \
    "ds_read_b64_tr_b16 v[194:195], v240 offset:6144\n\t" \
    "s_waitcnt lgkmcnt(6)\n\t" \
    "v_mfma_f32_16x16x32_bf16 v[92:95], v[196:199], v[172:175], v[92:95]\n\t" \
    "buffer_load_dwordx4 v250, %7, %4 offen offset:2048 lds\n\t" \
    "v_mfma_f32_16x16x32_bf16 v[88:91], v[196:199], v[168:171], v[88:91]\n\t" \
    "v_mfma_f32_16x16x32_bf16 v[84:87], v[196:199], v[164:167], v[84:87]\n\t" \
    "buffer_load_dwordx4 v251, %7, %4 offen offset:3072 lds\n\t" \
    "v_mfma_f32_16x16x32_bf16 v[80:83], v[196:199], v[160:163], v[80:83]\n\t" \
    "ds_read_b64_tr_b16 v[196:197], v241 offset:4096\n\t" \
    "ds_read_b64_tr_b16 v[198:199], v241 offset:6144\n\t" \
    "s_waitcnt lgkmcnt(6)\n\t" \
    "v_mfma_f32_16x16x32_bf16 v[96:99], v[200:203], v[160:163], v[96:99]\n\t" \
    "buffer_load_dwordx4 v[144:147], %13, %8, %5 offen offset:0 sc1\n\t" \
    "v_mfma_f32_16x16x32_bf16 v[100:103], v[200:203], v[164:167], v[100:103]\n\t" \
    "v_mfma_f32_16x16x32_bf16 v[104:107], v[200:203], v[168:171], v[104:107]\n\t" \
    "buffer_load_dwordx4 v[148:151], %13, %8, %5 offen offset:1024 sc1\n\t" \
    "v_mfma_f32_16x16x32_bf16 v[108:111], v[200:203], v[172:175], v[108:111]\n\t" \
    "ds_read_b64_tr_b16 v[200:201], v242 offset:4096\n\t" \
    "ds_read_b64_tr_b16 v[202:203], v242 offset:6144\n\t" \
    "s_waitcnt lgkmcnt(6)\n\t" \
    "v_mfma_f32_16x16x32_bf16 v[124:127], v[204:207], v[172:175], v[124:127]\n\t" \
    "buffer_load_dwordx4 v[152:155], %13, %8, %5 offen offset:2048 sc1\n\t" \
    "v_mfma_f32_16x16x32_bf16 v[120:123], v[204:207], v[168:171], v[120:123]\n\t" \
    "v_mfma_f32_16x16x32_bf16 v[116:119], v[204:207], v[164:167], v[116:119]\n\t" \
    "buffer_load_dwordx4 v[156:159], %13, %8, %5 offen offset:3072 sc1\n\t" \
    "s_add_u32 %3, %3, 1\n\t" \
    "s_cmp_eq_u32 %3, 12\n\t" \
    "s_cselect_b32 %3, 0, %3\n\t" \
    "v_mfma_f32_16x16x32_bf16 v[112:115], v[204:207], v[160:163], v[112:115]\n\t" \
    "ds_read_b64_tr_b16 v[204:205], v243 offset:4096\n\t" \
    "ds_read_b64_tr_b16 v[206:207], v243 offset:6144\n\t" \
    "s_waitcnt vmcnt(12)\n\t" \
    "s_waitcnt lgkmcnt(6)\n\t" \
    "v_mfma_f32_16x16x32_bf16 v[64:67], v[192:195], v[176:179], v[64:67]\n\t" \
    "s_add_u32 %4, %9, %3\n\t" \
    "s_cmp_ge_u32 %4, %23\n\t" \
    "s_cselect_b32 %5, 7, 0\n\t" \
    "s_add_u32 %4, %4, %5\n\t" \
    "s_lshl_b32 %5, %4, 12\n\t" \
    "s_add_u32 m0, %11, 0\n\t" \
    "s_lshl_b32 %4, %4, 15\n\t" \
    "buffer_load_dwordx4 %12, %7, %4 offen offset:0 lds\n\t" \
    "v_mfma_f32_16x16x32_bf16 v[68:71], v[192:195], v[180:183], v[68:71]\n\t" \
    "v_mfma_f32_16x16x32_bf16 v[72:75], v[192:195], v[184:187], v[72:75]\n\t" \
    "buffer_load_dwordx4 v249, %7, %4 offen offset:1024 lds\n\t" \
    "v_mfma_f32_16x16x32_bf16 v[76:79], v[192:195], v[188:191], v[76:79]\n\t" \
    "ds_read_b64_tr_b16 v[192:193], v240 offset:38912\n\t" \
    "ds_read_b64_tr_b16 v[194:195], v240 offset:40960\n\t" \
    "s_waitcnt lgkmcnt(6)\n\t" \
    "v_mfma_f32_16x16x32_bf16 v[92:95], v[196:199], v[188:191], v[92:95]\n\t" \
    "buffer_load_dwordx4 v250, %7, %4 offen offset:2048 lds\n\t" \
    "v_mfma_f32_16x16x32_bf16 v[88:91], v[196:199], v[184:187], v[88:91]\n\t" \
    "v_mfma_f32_16x16x32_bf16 v[84:87], v[196:199], v[180:183], v[84:87]\n\t" \
    "buffer_load_dwordx4 v251, %7, %4 offen offset:3072 lds\n\t" \
    "v_mfma_f32_16x16x32_bf16 v[80:83], v[196:199], v[176:179], v[80:83]\n\t" \
    "ds_read_b64_tr_b16 v[196:197], v241 offset:38912\n\t" \
    "ds_read_b64_tr_b16 v[198:199], v241 offset:40960\n\t" \
    "s_waitcnt lgkmcnt(6)\n\t" \
    "v_mfma_f32_16x16x32_bf16 v[96:99], v[200:203], v[176:179], v[96:99]\n\t" \
    "buffer_load_dwordx4 v[160:163], %13, %8, %5 offen offset:0 sc1\n\t" \
    "v_mfma_f32_16x16x32_bf16 v[100:103], v[200:203], v[180:183], v[100:103]\n\t" \
    "v_mfma_f32_16x16x32_bf16 v[104:107], v[200:203], v[184:187], v[104:107]\n\t" \
    "buffer_load_dwordx4 v[164:167], %13, %8, %5 offen offset:1024 sc1\n\t" \
    "v_mfma_f32_16x16x32_bf16 v[108:111], v[200:203], v[188:191], v[108:111]\n\t" \
    "ds_read_b64_tr_b16 v[200:201], v242 offset:38912\n\t" \
    "ds_read_b64_tr_b16 v[202:203], v242 offset:40960\n\t" \
    "s_waitcnt lgkmcnt(6)\n\t" \
    "v_mfma_f32_16x16x32_bf16 v[124:127], v[204:207], v[188:191], v[124:127]\n\t" \
    "buffer_load_dwordx4 v[168:171], %13, %8, %5 offen offset:2048 sc1\n\t" \
    "v_mfma_f32_16x16x32_bf16 v[120:123], v[204:207], v[184:187], v[120:123]\n\t" \
    "v_mfma_f32_16x16x32_bf16 v[116:119], v[204:207], v[180:183], v[116:119]\n\t" \
    "buffer_load_dwordx4 v[172:175], %13, %8, %5 offen offset:3072 sc1\n\t" \
    "s_add_u32 %3, %3, 1\n\t" \
    "s_cmp_eq_u32 %3, 12\n\t" \
    "s_cselect_b32 %3, 0, %3\n\t" \
    "v_mfma_f32_16x16x32_bf16 v[112:115], v[204:207], v[176:179], v[112:115]\n\t" \
    "ds_read_b64_tr_b16 v[204:205], v243 offset:38912\n\t" \
    "ds_read_b64_tr_b16 v[206:207], v243 offset:40960\n\t" \
    "s_waitcnt vmcnt(12)\n\t" \
    "s_waitcnt lgkmcnt(6)\n\t" \
    "v_mfma_f32_16x16x32_bf16 v[64:67], v[192:195], v[128:131], v[64:67]\n\t" \
    "s_add_u32 %4, %9, %3\n\t" \
    "s_cmp_ge_u32 %4, %23\n\t" \
    "s_cselect_b32 %5, 7, 0\n\t" \
    "s_add_u32 %4, %4, %5\n\t" \
    "s_lshl_b32 %5, %4, 12\n\t" \
    "s_add_u32 m0, %11, 4096\n\t" \
    "s_lshl_b32 %4, %4, 15\n\t" \
    "buffer_load_dwordx4 %12, %7, %4 offen offset:0 lds\n\t" \
    "v_mfma_f32_16x16x32_bf16 v[68:71], v[192:195], v[132:135], v[68:71]\n\t" \
    "v_mfma_f32_16x16x32_bf16 v[72:75], v[192:195], v[136:139], v[72:75]\n\t" \
    "buffer_load_dwordx4 v249, %7, %4 offen offset:1024 lds\n\t" \
    "v_mfma_f32_16x16x32_bf16 v[76:79], v[192:195], v[140:143], v[76:79]\n\t" \
    "ds_read_b64_tr_b16 v[192:193], v240 offset:43008\n\t" \
    "ds_read_b64_tr_b16 v[194:195], v240 offset:45056\n\t" \
    "s_waitcnt lgkmcnt(6)\n\t" \
    "v_mfma_f32_16x16x32_bf16 v[92:95], v[196:199], v[140:143], v[92:95]\n\t" \
    "buffer_load_dwordx4 v250, %7, %4 offen offset:2048 lds\n\t" \
    "v_mfma_f32_16x16x32_bf16 v[88:91], v[196:199], v[136:139], v[88:91]\n\t" \
    "v_mfma_f32_16x16x32_bf16 v[84:87], v[196:199], v[132:135], v[84:87]\n\t" \
    "buffer_load_dwordx4 v251, %7, %4 offen offset:3072 lds\n\t" \
    "v_mfma_f32_16x16x32_bf16 v[80:83], v[196:199], v[128:131], v[80:83]\n\t" \
    "ds_read_b64_tr_b16 v[196:197], v241 offset:43008\n\t" \
    "ds_read_b64_tr_b16 v[198:199], v241 offset:45056\n\t" \
    "s_waitcnt lgkmcnt(6)\n\t" \
    "v_mfma_f32_16x16x32_bf16 v[96:99], v[200:203], v[128:131], v[96:99]\n\t" \
    "buffer_load_dwordx4 v[176:179], %13, %8, %5 offen offset:0 sc1\n\t" \
    "v_mfma_f32_16x16x32_bf16 v[100:103], v[200:203], v[132:135], v[100:103]\n\t" \
    "v_mfma_f32_16x16x32_bf16 v[104:107], v[200:203], v[136:139], v[104:107]\n\t" \
    "buffer_load_dwordx4 v[180:183], %13, %8, %5 offen offset:1024 sc1\n\t" \
    "v_mfma_f32_16x16x32_bf16 v[108:111], v[200:203], v[140:143], v[108:111]\n\t" \
    "ds_read_b64_tr_b16 v[200:201], v242 offset:43008\n\t" \
    "ds_read_b64_tr_b16 v[202:203], v242 offset:45056\n\t" \
    "s_waitcnt lgkmcnt(6)\n\t" \
    "v_mfma_f32_16x16x32_bf16 v[124:127], v[204:207], v[140:143], v[124:127]\n\t" \
    "buffer_load_dwordx4 v[184:187], %13, %8, %5 offen offset:2048 sc1\n\t" \
    "v_mfma_f32_16x16x32_bf16 v[120:123], v[204:207], v[136:139], v[120:123]\n\t" \
    "v_mfma_f32_16x16x32_bf16 v[116:119], v[204:207], v[132:135], v[116:119]\n\t" \
    "buffer_load_dwordx4 v[188:191], %13, %8, %5 offen offset:3072 sc1\n\t" \
    "s_add_u32 %3, %3, 1\n\t" \
    "s_cmp_eq_u32 %3, 12\n\t" \
    "s_cselect_b32 %3, 0, %3\n\t" \
    "v_mfma_f32_16x16x32_bf16 v[112:115], v[204:207], v[128:131], v[112:115]\n\t" \
    "ds_read_b64_tr_b16 v[204:205], v243 offset:43008\n\t" \
    "ds_read_b64_tr_b16 v[206:207], v243 offset:45056\n\t" \
    "s_waitcnt vmcnt(12)\n\t" \
    "s_waitcnt lgkmcnt(6)\n\t" \
    "v_mfma_f32_16x16x32_bf16 v[64:67], v[192:195], v[144:147], v[64:67]\n\t" \
    "s_add_u32 %4, %9, %3\n\t" \
    "s_cmp_ge_u32 %4, %23\n\t" \
    "s_cselect_b32 %5, 7, 0\n\t" \
    "s_add_u32 %4, %4, %5\n\t" \
    "s_lshl_b32 %5, %4, 12\n\t" \
    "s_add_u32 m0, %10, 0\n\t" \
    "s_lshl_b32 %4, %4, 15\n\t" \
    "buffer_load_dwordx4 %12, %7, %4 offen offset:0 lds\n\t" \
    "v_mfma_f32_16x16x32_bf16 v[68:71], v[192:195], v[148:151], v[68:71]\n\t" \
    "v_mfma_f32_16x16x32_bf16 v[72:75], v[192:195], v[152:155], v[72:75]\n\t" \
    "buffer_load_dwordx4 v249, %7, %4 offen offset:1024 lds\n\t" \
    "v_mfma_f32_16x16x32_bf16 v[76:79], v[192:195], v[156:159], v[76:79]\n\t" \
    "ds_read_b64_tr_b16 v[192:193], v240 offset:0\n\t" \
    "ds_read_b64_tr_b16 v[194:195], v240 offset:2048\n\t" \
    "s_waitcnt lgkmcnt(6)\n\t" \
    "v_mfma_f32_16x16x32_bf16 v[92:95], v[196:199], v[156:159], v[92:95]\n\t" \
    "buffer_load_dwordx4 v250, %7, %4 offen offset:2048 lds\n\t" \
    "v_mfma_f32_16x16x32_bf16 v[88:91], v[196:199], v[152:155], v[88:91]\n\t" \
    "v_mfma_f32_16x16x32_bf16 v[84:87], v[196:199], v[148:151], v[84:87]\n\t" \
    "buffer_load_dwordx4 v251, %7, %4 offen offset:3072 lds\n\t" \
    "v_mfma_f32_16x16x32_bf16 v[80:83], v[196:199], v[144:147], v[80:83]\n\t" \
    "ds_read_b64_tr_b16 v[196:197], v241 offset:0\n\t" \
    "ds_read_b64_tr_b16 v[198:199], v241 offset:2048\n\t" \
    "s_waitcnt lgkmcnt(6)\n\t" \
    "v_mfma_f32_16x16x32_bf16 v[96:99], v[200:203], v[144:147], v[96:99]\n\t" \
    "buffer_load_dwordx4 v[128:131], %13, %8, %5 offen offset:0 sc1\n\t" \
    "v_mfma_f32_16x16x32_bf16 v[100:103], v[200:203], v[148:151], v[100:103]\n\t" \
    "v_mfma_f32_16x16x32_bf16 v[104:107], v[200:203], v[152:155], v[104:107]\n\t" \
    "buffer_load_dwordx4 v[132:135], %13, %8, %5 offen offset:1024 sc1\n\t" \
    "v_mfma_f32_16x16x32_bf16 v[108:111], v[200:203], v[156:159], v[108:111]\n\t" \
    "ds_read_b64_tr_b16 v[200:201], v242 offset:0\n\t" \
    "ds_read_b64_tr_b16 v[202:203], v242 offset:2048\n\t" \
    "s_waitcnt lgkmcnt(6)\n\t" \
    "v_mfma_f32_16x16x32_bf16 v[124:127], v[204:207], v[156:159], v[124:127]\n\t" \
    "buffer_load_dwordx4 v[136:139], %13, %8, %5 offen offset:2048 sc1\n\t" \
    "v_mfma_f32_16x16x32_bf16 v[120:123], v[204:207], v[152:155], v[120:123]\n\t" \
    "v_mfma_f32_16x16x32_bf16 v[116:119], v[204:207], v[148:151], v[116:119]\n\t" \
    "buffer_load_dwordx4 v[140:143], %13, %8, %5 offen offset:3072 sc1\n\t" \
    "s_add_u32 %3, %3, 1\n\t" \
    "s_cmp_eq_u32 %3, 12\n\t" \
    "s_cselect_b32 %3, 0, %3\n\t" \
    "v_mfma_f32_16x16x32_bf16 v[112:115], v[204:207], v[144:147], v[112:115]\n\t" \
    "ds_read_b64_tr_b16 v[204:205], v243 offset:0\n\t" \
    "ds_read_b64_tr_b16 v[206:207], v243 offset:2048\n\t" \
    "s_waitcnt vmcnt(12)\n\t" \
    "s_waitcnt lgkmcnt(6)\n\t" \
    "v_mfma_f32_16x16x32_bf16 v[64:67], v[192:195], v[160:163], v[64:67]\n\t" \
    "s_add_u32 %4, %9, %3\n\t" \
    "s_cmp_ge_u32 %4, %23\n\t" \
    "s_cselect_b32 %5, 7, 0\n\t" \
    "s_add_u32 %4, %4, %5\n\t" \
    "s_lshl_b32 %5, %4, 12\n\t" \
    "s_add_u32 m0, %10, 4096\n\t" \
    "s_lshl_b32 %4, %4, 15\n\t" \
    "buffer_load_dwordx4 %12, %7, %4 offen offset:0 lds\n\t" \
    "v_mfma_f32_16x16x32_bf16 v[68:71], v[192:195], v[164:167], v[68:71]\n\t" \
    "v_mfma_f32_16x16x32_bf16 v[72:75], v[192:195], v[168:171], v[72:75]\n\t" \
    "buffer_load_dwordx4 v249, %7, %4 offen offset:1024 lds\n\t" \
    "v_mfma_f32_16x16x32_bf16 v[76:79], v[192:195], v[172:175], v[76:79]\n\t" \
    "ds_read_b64_tr_b16 v[192:193], v240 offset:4096\n\t" \
    "ds_read_b64_tr_b16 v[194:195], v240 offset:6144\n\t" \
    "s_waitcnt lgkmcnt(6)\n\t" \
    "v_mfma_f32_16x16x32_bf16 v[92:95], v[196:199], v[172:175], v[92:95]\n\t" \
    "buffer_load_dwordx4 v250, %7, %4 offen offset:2048 lds\n\t" \
    "v_mfma_f32_16x16x32_bf16 v[88:91], v[196:199], v[168:171], v[88:91]\n\t" \
    "v_mfma_f32_16x16x32_bf16 v[84:87], v[196:199], v[164:167], v[84:87]\n\t" \
    "buffer_load_dwordx4 v251, %7, %4 offen offset:3072 lds\n\t" \
    "v_mfma_f32_16x16x32_bf16 v[80:83], v[196:199], v[160:163], v[80:83]\n\t" \
    "ds_read_b64_tr_b16 v[196:197], v241 offset:4096\n\t" \
    "ds_read_b64_tr_b16 v[198:199], v241 offset:6144\n\t" \
    "s_waitcnt lgkmcnt(6)\n\t" \
    "v_mfma_f32_16x16x32_bf16 v[96:99], v[200:203], v[160:163], v[96:99]\n\t" \
    "buffer_load_dwordx4 v[144:147], %13, %8, %5 offen offset:0 sc1\n\t" \
    "v_mfma_f32_16x16x32_bf16 v[100:103], v[200:203], v[164:167], v[100:103]\n\t" \
    "v_mfma_f32_16x16x32_bf16 v[104:107], v[200:203], v[168:171], v[104:107]\n\t" \
    "buffer_load_dwordx4 v[148:151], %13, %8, %5 offen offset:1024 sc1\n\t" \
    "v_mfma_f32_16x16x32_bf16 v[108:111], v[200:203], v[172:175], v[108:111]\n\t" \
    "ds_read_b64_tr_b16 v[200:201], v242 offset:4096\n\t" \
    "ds_read_b64_tr_b16 v[202:203], v242 offset:6144\n\t" \
    "s_waitcnt lgkmcnt(6)\n\t" \
    "v_mfma_f32_16x16x32_bf16 v[124:127], v[204:207], v[172:175], v[124:127]\n\t" \
    "buffer_load_dwordx4 v[152:155], %13, %8, %5 offen offset:2048 sc1\n\t" \
    "v_mfma_f32_16x16x32_bf16 v[120:123], v[204:207], v[168:171], v[120:123]\n\t" \
    "v_mfma_f32_16x16x32_bf16 v[116:119], v[204:207], v[164:167], v[116:119]\n\t" \
    "buffer_load_dwordx4 v[156:159], %13, %8, %5 offen offset:3072 sc1\n\t" \
    "s_add_u32 %3, %3, 1\n\t" \
    "s_cmp_eq_u32 %3, 12\n\t" \
    "s_cselect_b32 %3, 0, %3\n\t" \
    "v_mfma_f32_16x16x32_bf16 v[112:115], v[204:207], v[160:163], v[112:115]\n\t" \
    "ds_read_b64_tr_b16 v[204:205], v243 offset:4096\n\t" \
    "ds_read_b64_tr_b16 v[206:207], v243 offset:6144\n\t" \
    "s_waitcnt vmcnt(12)\n\t" \
    "s_waitcnt lgkmcnt(6)\n\t" \
    "v_mfma_f32_16x16x32_bf16 v[64:67], v[192:195], v[176:179], v[64:67]\n\t" \
    "s_add_u32 %4, %9, %3\n\t" \
    "s_cmp_ge_u32 %4, %23\n\t" \
    "s_cselect_b32 %5, 7, 0\n\t" \
    "s_add_u32 %4, %4, %5\n\t" \
    "s_lshl_b32 %5, %4, 12\n\t" \
    "s_add_u32 m0, %11, 0\n\t" \
    "s_lshl_b32 %4, %4, 15\n\t" \
    "buffer_load_dwordx4 %12, %7, %4 offen offset:0 lds\n\t" \
    "v_mfma_f32_16x16x32_bf16 v[68:71], v[192:195], v[180:183], v[68:71]\n\t" \
    "v_mfma_f32_16x16x32_bf16 v[72:75], v[192:195], v[184:187], v[72:75]\n\t" \
    "buffer_load_dwordx4 v249, %7, %4 offen offset:1024 lds\n\t" \
    "v_mfma_f32_16x16x32_bf16 v[76:79], v[192:195], v[188:191], v[76:79]\n\t" \
    "ds_read_b64_tr_b16 v[192:193], v240 offset:38912\n\t" \
    "ds_read_b64_tr_b16 v[194:195], v240 offset:40960\n\t" \
    "s_waitcnt lgkmcnt(6)\n\t" \
    "v_mfma_f32_16x16x32_bf16 v[92:95], v[196:199], v[188:191], v[92:95]\n\t" \
    "buffer_load_dwordx4 v250, %7, %4 offen offset:2048 lds\n\t" \
    "v_mfma_f32_16x16x32_bf16 v[88:91], v[196:199], v[184:187], v[88:91]\n\t" \
    "v_mfma_f32_16x16x32_bf16 v[84:87], v[196:199], v[180:183], v[84:87]\n\t" \
    "buffer_load_dwordx4 v251, %7, %4 offen offset:3072 lds\n\t" \
    "v_mfma_f32_16x16x32_bf16 v[80:83], v[196:199], v[176:179], v[80:83]\n\t" \
    "ds_read_b64_tr_b16 v[196:197], v241 offset:38912\n\t" \
    "ds_read_b64_tr_b16 v[198:199], v241 offset:40960\n\t" \
    "s_waitcnt lgkmcnt(6)\n\t" \
    "v_mfma_f32_16x16x32_bf16 v[96:99], v[200:203], v[176:179], v[96:99]\n\t" \
    "buffer_load_dwordx4 v[160:163], %13, %8, %5 offen offset:0 sc1\n\t" \
    "v_mfma_f32_16x16x32_bf16 v[100:103], v[200:203], v[180:183], v[100:103]\n\t" \
    "v_mfma_f32_16x16x32_bf16 v[104:107], v[200:203], v[184:187], v[104:107]\n\t" \
    "buffer_load_dwordx4 v[164:167], %13, %8, %5 offen offset:1024 sc1\n\t" \
    "v_mfma_f32_16x16x32_bf16 v[108:111], v[200:203], v[188:191], v[108:111]\n\t" \
    "ds_read_b64_tr_b16 v[200:201], v242 offset:38912\n\t" \
    "ds_read_b64_tr_b16 v[202:203], v242 offset:40960\n\t" \
    "s_waitcnt lgkmcnt(6)\n\t" \
    "v_mfma_f32_16x16x32_bf16 v[124:127], v[204:207], v[188:191], v[124:127]\n\t" \
    "buffer_load_dwordx4 v[168:171], %13, %8, %5 offen offset:2048 sc1\n\t" \
    "v_mfma_f32_16x16x32_bf16 v[120:123], v[204:207], v[184:187], v[120:123]\n\t" \
    "v_mfma_f32_16x16x32_bf16 v[116:119], v[204:207], v[180:183], v[116:119]\n\t" \
    "buffer_load_dwordx4 v[172:175], %13, %8, %5 offen offset:3072 sc1\n\t" \
    "s_add_u32 %3, %3, 1\n\t" \
    "s_cmp_eq_u32 %3, 12\n\t" \
    "s_cselect_b32 %3, 0, %3\n\t" \
    "v_mfma_f32_16x16x32_bf16 v[112:115], v[204:207], v[176:179], v[112:115]\n\t" \
    "ds_read_b64_tr_b16 v[204:205], v243 offset:38912\n\t" \
    "ds_read_b64_tr_b16 v[206:207], v243 offset:40960\n\t" \
    "s_waitcnt vmcnt(12)\n\t" \
    "s_waitcnt lgkmcnt(6)\n\t" \
    "v_mfma_f32_16x16x32_bf16 v[64:67], v[192:195], v[128:131], v[64:67]\n\t" \
    "s_add_u32 %4, %9, %3\n\t" \
    "s_cmp_ge_u32 %4, %23\n\t" \
    "s_cselect_b32 %5, 7, 0\n\t" \
    "s_add_u32 %4, %4, %5\n\t" \
    "s_lshl_b32 %5, %4, 12\n\t" \
    "s_add_u32 m0, %11, 4096\n\t" \
    "s_lshl_b32 %4, %4, 15\n\t" \
    "buffer_load_dwordx4 %12, %7, %4 offen offset:0 lds\n\t" \
    "v_mfma_f32_16x16x32_bf16 v[68:71], v[192:195], v[132:135], v[68:71]\n\t" \
    "v_mfma_f32_16x16x32_bf16 v[72:75], v[192:195], v[136:139], v[72:75]\n\t" \
    "buffer_load_dwordx4 v249, %7, %4 offen offset:1024 lds\n\t" \
    "v_mfma_f32_16x16x32_bf16 v[76:79], v[192:195], v[140:143], v[76:79]\n\t" \
    "ds_read_b64_tr_b16 v[192:193], v240 offset:43008\n\t" \
    "ds_read_b64_tr_b16 v[194:195], v240 offset:45056\n\t" \
    "s_waitcnt lgkmcnt(6)\n\t" \
    "v_mfma_f32_16x16x32_bf16 v[92:95], v[196:199], v[140:143], v[92:95]\n\t" \
    "buffer_load_dwordx4 v250, %7, %4 offen offset:2048 lds\n\t" \
    "v_mfma_f32_16x16x32_bf16 v[88:91], v[196:199], v[136:139], v[88:91]\n\t" \
    "v_mfma_f32_16x16x32_bf16 v[84:87], v[196:199], v[132:135], v[84:87]\n\t" \
    "buffer_load_dwordx4 v251, %7, %4 offen offset:3072 lds\n\t" \
    "v_mfma_f32_16x16x32_bf16 v[80:83], v[196:199], v[128:131], v[80:83]\n\t" \
    "ds_read_b64_tr_b16 v[196:197], v241 offset:43008\n\t" \
    "ds_read_b64_tr_b16 v[198:199], v241 offset:45056\n\t" \
    "s_waitcnt lgkmcnt(6)\n\t" \
    "v_mfma_f32_16x16x32_bf16 v[96:99], v[200:203], v[128:131], v[96:99]\n\t" \
    "buffer_load_dwordx4 v[176:179], %13, %8, %5 offen offset:0 sc1\n\t" \
    "v_mfma_f32_16x16x32_bf16 v[100:103], v[200:203], v[132:135], v[100:103]\n\t" \
    "v_mfma_f32_16x16x32_bf16 v[104:107], v[200:203], v[136:139], v[104:107]\n\t" \
    "buffer_load_dwordx4 v[180:183], %13, %8, %5 offen offset:1024 sc1\n\t" \
    "v_mfma_f32_16x16x32_bf16 v[108:111], v[200:203], v[140:143], v[108:111]\n\t" \
    "ds_read_b64_tr_b16 v[200:201], v242 offset:43008\n\t" \
    "ds_read_b64_tr_b16 v[202:203], v242 offset:45056\n\t" \
    "s_waitcnt lgkmcnt(6)\n\t" \
    "v_mfma_f32_16x16x32_bf16 v[124:127], v[204:207], v[140:143], v[124:127]\n\t" \
    "buffer_load_dwordx4 v[184:187], %13, %8, %5 offen offset:2048 sc1\n\t" \
    "v_mfma_f32_16x16x32_bf16 v[120:123], v[204:207], v[136:139], v[120:123]\n\t" \
    "v_mfma_f32_16x16x32_bf16 v[116:119], v[204:207], v[132:135], v[116:119]\n\t" \
    "buffer_load_dwordx4 v[188:191], %13, %8, %5 offen offset:3072 sc1\n\t" \
    "s_add_u32 %3, %3, 1\n\t" \
    "s_cmp_eq_u32 %3, 12\n\t" \
    "s_cselect_b32 %3, 0, %3\n\t" \
    "v_mfma_f32_16x16x32_bf16 v[112:115], v[204:207], v[128:131], v[112:115]\n\t" \
    "ds_read_b64_tr_b16 v[204:205], v243 offset:43008\n\t" \
    "ds_read_b64_tr_b16 v[206:207], v243 offset:45056\n\t" \
    "s_waitcnt vmcnt(12)\n\t" \
    "s_waitcnt lgkmcnt(6)\n\t" \
    "v_mfma_f32_16x16x32_bf16 v[64:67], v[192:195], v[144:147], v[64:67]\n\t" \
    "s_add_u32 m0, %10, 0\n\t" \
    "s_add_u32 %4, %21, 0\n\t" \
    "buffer_load_dwordx4 %20, %19, %4 offen lds\n\t" \
    "v_mfma_f32_16x16x32_bf16 v[68:71], v[192:195], v[148:151], v[68:71]\n\t" \
    "v_mfma_f32_16x16x32_bf16 v[72:75], v[192:195], v[152:155], v[72:75]\n\t" \
    "s_add_u32 m0, %10, 1024\n\t" \
    "s_add_u32 %4, %21, 8192\n\t" \
    "buffer_load_dwordx4 %20, %19, %4 offen lds\n\t" \
    "v_mfma_f32_16x16x32_bf16 v[76:79], v[192:195], v[156:159], v[76:79]\n\t" \
    "ds_read_b64_tr_b16 v[192:193], v240 offset:0\n\t" \
    "ds_read_b64_tr_b16 v[194:195], v240 offset:2048\n\t" \
    "s_waitcnt lgkmcnt(6)\n\t" \
    "v_mfma_f32_16x16x32_bf16 v[92:95], v[196:199], v[156:159], v[92:95]\n\t" \
    "s_add_u32 m0, %10, 2048\n\t" \
    "s_add_u32 %4, %21, 16384\n\t" \
    "buffer_load_dwordx4 %20, %19, %4 offen lds\n\t" \
    "v_mfma_f32_16x16x32_bf16 v[88:91], v[196:199], v[152:155], v[88:91]\n\t" \
    "v_mfma_f32_16x16x32_bf16 v[84:87], v[196:199], v[148:151], v[84:87]\n\t" \
    "s_add_u32 m0, %10, 3072\n\t" \
    "s_add_u32 %4, %21, 24576\n\t" \
    "buffer_load_dwordx4 %20, %19, %4 offen lds\n\t" \
    "v_mfma_f32_16x16x32_bf16 v[80:83], v[196:199], v[144:147], v[80:83]\n\t" \
    "ds_read_b64_tr_b16 v[196:197], v241 offset:0\n\t" \
    "ds_read_b64_tr_b16 v[198:199], v241 offset:2048\n\t" \
    "s_waitcnt lgkmcnt(6)\n\t" \
    "v_mfma_f32_16x16x32_bf16 v[96:99], v[200:203], v[144:147], v[96:99]\n\t" \
    "v_mfma_f32_16x16x32_bf16 v[100:103], v[200:203], v[148:151], v[100:103]\n\t" \
    "v_mfma_f32_16x16x32_bf16 v[104:107], v[200:203], v[152:155], v[104:107]\n\t" \
    "v_mfma_f32_16x16x32_bf16 v[108:111], v[200:203], v[156:159], v[108:111]\n\t" \
    "ds_read_b64_tr_b16 v[200:201], v242 offset:0\n\t" \
    "ds_read_b64_tr_b16 v[202:203], v242 offset:2048\n\t" \
    "s_waitcnt lgkmcnt(6)\n\t" \
    "v_mfma_f32_16x16x32_bf16 v[124:127], v[204:207], v[156:159], v[124:127]\n\t" \
    "v_mfma_f32_16x16x32_bf16 v[120:123], v[204:207], v[152:155], v[120:123]\n\t" \
    "v_mfma_f32_16x16x32_bf16 v[116:119], v[204:207], v[148:151], v[116:119]\n\t" \
    "v_mfma_f32_16x16x32_bf16 v[112:115], v[204:207], v[144:147], v[112:115]\n\t" \
    "ds_read_b64_tr_b16 v[204:205], v243 offset:0\n\t" \
    "ds_read_b64_tr_b16 v[206:207], v243 offset:2048\n\t" \
    "s_waitcnt vmcnt(8)\n\t" \
    "s_waitcnt lgkmcnt(6)\n\t" \
    "v_mfma_f32_16x16x32_bf16 v[64:67], v[192:195], v[160:163], v[64:67]\n\t" \
    "s_add_u32 m0, %10, 4096\n\t" \
    "s_add_u32 %4, %21, 32768\n\t" \
    "buffer_load_dwordx4 %20, %19, %4 offen lds\n\t" \
    "v_mfma_f32_16x16x32_bf16 v[68:71], v[192:195], v[164:167], v[68:71]\n\t" \
    "v_mfma_f32_16x16x32_bf16 v[72:75], v[192:195], v[168:171], v[72:75]\n\t" \
    "s_add_u32 m0, %10, 5120\n\t" \
    "s_add_u32 %4, %21, 40960\n\t" \
    "buffer_load_dwordx4 %20, %19, %4 offen lds\n\t" \
    "v_mfma_f32_16x16x32_bf16 v[76:79], v[192:195], v[172:175], v[76:79]\n\t" \
    "ds_read_b64_tr_b16 v[192:193], v240 offset:4096\n\t" \
    "ds_read_b64_tr_b16 v[194:195], v240 offset:6144\n\t" \
    "s_waitcnt lgkmcnt(6)\n\t" \
    "v_mfma_f32_16x16x32_bf16 v[92:95], v[196:199], v[172:175], v[92:95]\n\t" \
    "s_add_u32 m0, %10, 6144\n\t" \
    "s_add_u32 %4, %21, 49152\n\t" \
    "buffer_load_dwordx4 %20, %19, %4 offen lds\n\t" \
    "v_mfma_f32_16x16x32_bf16 v[88:91], v[196:199], v[168:171], v[88:91]\n\t" \
    "v_mfma_f32_16x16x32_bf16 v[84:87], v[196:199], v[164:167], v[84:87]\n\t" \
    "s_add_u32 m0, %10, 7168\n\t" \
    "s_add_u32 %4, %21, 57344\n\t" \
    "buffer_load_dwordx4 %20, %19, %4 offen lds\n\t" \
    "v_mfma_f32_16x16x32_bf16 v[80:83], v[196:199], v[160:163], v[80:83]\n\t" \
    "ds_read_b64_tr_b16 v[196:197], v241 offset:4096\n\t" \
    "ds_read_b64_tr_b16 v[198:199], v241 offset:6144\n\t" \
    "s_waitcnt lgkmcnt(6)\n\t" \
    "v_mfma_f32_16x16x32_bf16 v[96:99], v[200:203], v[160:163], v[96:99]\n\t" \
    "v_mfma_f32_16x16x32_bf16 v[100:103], v[200:203], v[164:167], v[100:103]\n\t" \
    "v_mfma_f32_16x16x32_bf16 v[104:107], v[200:203], v[168:171], v[104:107]\n\t" \
    "v_mfma_f32_16x16x32_bf16 v[108:111], v[200:203], v[172:175], v[108:111]\n\t" \
    "ds_read_b64_tr_b16 v[200:201], v242 offset:4096\n\t" \
    "ds_read_b64_tr_b16 v[202:203], v242 offset:6144\n\t" \
    "s_waitcnt lgkmcnt(6)\n\t" \
    "v_mfma_f32_16x16x32_bf16 v[124:127], v[204:207], v[172:175], v[124:127]\n\t" \
    "v_mfma_f32_16x16x32_bf16 v[120:123], v[204:207], v[168:171], v[120:123]\n\t" \
    "v_mfma_f32_16x16x32_bf16 v[116:119], v[204:207], v[164:167], v[116:119]\n\t" \
    "v_mfma_f32_16x16x32_bf16 v[112:115], v[204:207], v[160:163], v[112:115]\n\t" \
    "ds_read_b64_tr_b16 v[204:205], v243 offset:4096\n\t" \
    "ds_read_b64_tr_b16 v[206:207], v243 offset:6144\n\t" \
    "s_waitcnt vmcnt(8)\n\t" \
    "s_waitcnt lgkmcnt(6)\n\t" \
    "v_mfma_f32_16x16x32_bf16 v[64:67], v[192:195], v[176:179], v[64:67]\n\t" \
    "v_mfma_f32_16x16x32_bf16 v[68:71], v[192:195], v[180:183], v[68:71]\n\t" \
    "v_mfma_f32_16x16x32_bf16 v[72:75], v[192:195], v[184:187], v[72:75]\n\t" \
    "v_mfma_f32_16x16x32_bf16 v[76:79], v[192:195], v[188:191], v[76:79]\n\t" \
    "s_waitcnt lgkmcnt(4)\n\t" \
    "v_mfma_f32_16x16x32_bf16 v[92:95], v[196:199], v[188:191], v[92:95]\n\t" \
    "v_mfma_f32_16x16x32_bf16 v[88:91], v[196:199], v[184:187], v[88:91]\n\t" \
    "v_mfma_f32_16x16x32_bf16 v[84:87], v[196:199], v[180:183], v[84:87]\n\t" \
    "v_mfma_f32_16x16x32_bf16 v[80:83], v[196:199], v[176:179], v[80:83]\n\t" \
    "s_waitcnt lgkmcnt(2)\n\t" \
    "v_mfma_f32_16x16x32_bf16 v[96:99], v[200:203], v[176:179], v[96:99]\n\t" \
    "v_mfma_f32_16x16x32_bf16 v[100:103], v[200:203], v[180:183], v[100:103]\n\t" \
    "v_mfma_f32_16x16x32_bf16 v[104:107], v[200:203], v[184:187], v[104:107]\n\t" \
    "v_mfma_f32_16x16x32_bf16 v[108:111], v[200:203], v[188:191], v[108:111]\n\t" \
    "s_waitcnt lgkmcnt(0)\n\t" \
    "v_mfma_f32_16x16x32_bf16 v[124:127], v[204:207], v[188:191], v[124:127]\n\t" \
    "v_mfma_f32_16x16x32_bf16 v[120:123], v[204:207], v[184:187], v[120:123]\n\t" \
    "v_mfma_f32_16x16x32_bf16 v[116:119], v[204:207], v[180:183], v[116:119]\n\t" \
    "v_mfma_f32_16x16x32_bf16 v[112:115], v[204:207], v[176:179], v[112:115]\n\t" \
    "s_setprio 2\n\t" \
    "s_waitcnt vmcnt(0)\n\t" \
    "ds_read_b64_tr_b16 v[204:205], v244 offset:0\n\t" \
    "ds_read_b64_tr_b16 v[206:207], v244 offset:1024\n\t" \
    "ds_read_b64_tr_b16 v[208:209], v245 offset:0\n\t" \
    "ds_read_b64_tr_b16 v[210:211], v245 offset:1024\n\t" \
    "ds_read_b64_tr_b16 v[212:213], v246 offset:0\n\t" \
    "ds_read_b64_tr_b16 v[214:215], v246 offset:1024\n\t" \
    "ds_read_b64_tr_b16 v[216:217], v247 offset:0\n\t" \
    "ds_read_b64_tr_b16 v[218:219], v247 offset:1024\n\t" \
    "ds_read_b64_tr_b16 v[220:221], v244 offset:4096\n\t" \
    "ds_read_b64_tr_b16 v[222:223], v244 offset:5120\n\t" \
    "ds_read_b64_tr_b16 v[224:225], v245 offset:4096\n\t" \
    "ds_read_b64_tr_b16 v[226:227], v245 offset:5120\n\t" \
    "ds_read_b64_tr_b16 v[228:229], v246 offset:4096\n\t" \
    "ds_read_b64_tr_b16 v[230:231], v246 offset:5120\n\t" \
    "ds_read_b64_tr_b16 v[232:233], v247 offset:4096\n\t" \
    "ds_read_b64_tr_b16 v[234:235], v247 offset:5120\n\t" \
    "s_waitcnt lgkmcnt(0)\n\t" \
    "s_nop 7\n\t" \
    "s_branch 92f\n\t" \
    "91:\n\t" \
    "s_add_u32 %4, %15, 0\n\t" \
    "s_and_b32 %4, %4, 3\n\t" \
    "s_lshl_b32 %4, %4, 5\n\t" \
    "v_xor_b32 v240, %4, %14\n\t" \
    "v_add_u32 v244, %4, %22\n\t" \
    "v_add_u32 v240, %11, v240\n\t" \
    "s_add_u32 %4, %15, 1\n\t" \
    "s_and_b32 %4, %4, 3\n\t" \
    "s_lshl_b32 %4, %4, 5\n\t" \
    "v_xor_b32 v241, %4, %14\n\t" \
    "v_add_u32 v245, %4, %22\n\t" \
    "v_add_u32 v241, %11, v241\n\t" \
    "s_add_u32 %4, %15, 2\n\t" \
    "s_and_b32 %4, %4, 3\n\t" \
    "s_lshl_b32 %4, %4, 5\n\t" \
    "v_xor_b32 v242, %4, %14\n\t" \
    "v_add_u32 v246, %4, %22\n\t" \
    "v_add_u32 v242, %11, v242\n\t" \
    "s_add_u32 %4, %15, 3\n\t" \
    "s_and_b32 %4, %4, 3\n\t" \
    "s_lshl_b32 %4, %4, 5\n\t" \
    "v_xor_b32 v243, %4, %14\n\t" \
    "v_add_u32 v247, %4, %22\n\t" \
    "v_add_u32 v243, %11, v243\n\t" \
    "v_add_u32 v249, 7168, %12\n\t" \
    "v_add_u32 v250, 14336, %12\n\t" \
    "v_add_u32 v251, 21504, %12\n\t" \
    "s_mov_b32 vcc_hi, %3\n\t" \
    "s_add_u32 %4, %9, %3\n\t" \
    "s_cmp_ge_u32 %4, %23\n\t" \
    "s_cselect_b32 %5, 7, 0\n\t" \
    "s_add_u32 %4, %4, %5\n\t" \
    "s_lshl_b32 %5, %4, 12\n\t" \
    "s_add_u32 m0, %11, 4096\n\t" \
    "s_lshl_b32 %4, %4, 15\n\t" \
    "buffer_load_dwordx4 %12, %7, %4 offen offset:0 lds\n\t" \
    "buffer_load_dwordx4 v249, %7, %4 offen offset:1024 lds\n\t" \
    "buffer_load_dwordx4 v250, %7, %4 offen offset:2048 lds\n\t" \
    "buffer_load_dwordx4 v251, %7, %4 offen offset:3072 lds\n\t" \
    "s_add_u32 %3, %3, 1\n\t" \
    "s_cmp_eq_u32 %3, 13\n\t" \
    "s_cselect_b32 %3, 0, %3\n\t" \
    "s_add_u32 %4, %9, %3\n\t" \
    "s_cmp_ge_u32 %4, %23\n\t" \
    "s_cselect_b32 %5, 7, 0\n\t" \
    "s_add_u32 %4, %4, %5\n\t" \
    "s_lshl_b32 %5, %4, 12\n\t" \
    "s_add_u32 m0, %10, 0\n\t" \
    "s_lshl_b32 %4, %4, 15\n\t" \
    "buffer_load_dwordx4 %12, %7, %4 offen offset:0 lds\n\t" \
    "buffer_load_dwordx4 v249, %7, %4 offen offset:1024 lds\n\t" \
    "buffer_load_dwordx4 v250, %7, %4 offen offset:2048 lds\n\t" \
    "buffer_load_dwordx4 v251, %7, %4 offen offset:3072 lds\n\t" \
    "s_add_u32 %3, %3, 1\n\t" \
    "s_cmp_eq_u32 %3, 13\n\t" \
    "s_cselect_b32 %3, 0, %3\n\t" \
    "s_add_u32 %4, %9, %3\n\t" \
    "s_cmp_ge_u32 %4, %23\n\t" \
    "s_cselect_b32 %5, 7, 0\n\t" \
    "s_add_u32 %4, %4, %5\n\t" \
    "s_lshl_b32 %5, %4, 12\n\t" \
    "s_add_u32 m0, %10, 4096\n\t" \
    "s_lshl_b32 %4, %4, 15\n\t" \
    "buffer_load_dwordx4 %12, %7, %4 offen offset:0 lds\n\t" \
    "buffer_load_dwordx4 v249, %7, %4 offen offset:1024 lds\n\t" \
    "buffer_load_dwordx4 v250, %7, %4 offen offset:2048 lds\n\t" \
    "buffer_load_dwordx4 v251, %7, %4 offen offset:3072 lds\n\t" \
    "s_add_u32 %3, %3, 1\n\t" \
    "s_cmp_eq_u32 %3, 13\n\t" \
    "s_cselect_b32 %3, 0, %3\n\t" \
    "s_mov_b32 %6, 0\n\t" \
    "1:\n\t" \
    "buffer_load_dwordx2 v[236:237], %17, %16, 0 offen sc1\n\t" \
    "s_waitcnt vmcnt(0)\n\t" \
    "v_cmp_eq_u64 s[64:65], %18, v[236:237]\n\t" \
    "s_add_u32 %6, %6, 1\n\t" \
    "s_cmp_eq_u64 s[64:65], -1\n\t" \
    "s_cbranch_scc1 10f\n\t" \
    "s_sleep 4\n\t" \
    "s_cmp_lt_u32 %6, 0x400000\n\t" \
    "s_cbranch_scc1 1b\n\t" \
    "10:\n\t" \
    "s_mov_b32 %3, vcc_hi\n\t" \
    "s_add_u32 %4, %9, %3\n\t" \
    "s_cmp_ge_u32 %4, %23\n\t" \
    "s_cselect_b32 %5, 7, 0\n\t" \
    "s_add_u32 %4, %4, %5\n\t" \
    "s_lshl_b32 %5, %4, 12\n\t" \
    "buffer_load_dwordx4 v[128:131], %13, %8, %5 offen offset:0 sc1\n\t" \
    "buffer_load_dwordx4 v[132:135], %13, %8, %5 offen offset:1024 sc1\n\t" \
    "buffer_load_dwordx4 v[136:139], %13, %8, %5 offen offset:2048 sc1\n\t" \
    "buffer_load_dwordx4 v[140:143], %13, %8, %5 offen offset:3072 sc1\n\t" \
    "s_add_u32 %3, %3, 1\n\t" \
    "s_cmp_eq_u32 %3, 13\n\t" \
    "s_cselect_b32 %3, 0, %3\n\t" \
    "s_add_u32 %4, %9, %3\n\t" \
    "s_cmp_ge_u32 %4, %23\n\t" \
    "s_cselect_b32 %5, 7, 0\n\t" \
    "s_add_u32 %4, %4, %5\n\t" \
    "s_lshl_b32 %5, %4, 12\n\t" \
    "buffer_load_dwordx4 v[144:147], %13, %8, %5 offen offset:0 sc1\n\t" \
    "buffer_load_dwordx4 v[148:151], %13, %8, %5 offen offset:1024 sc1\n\t" \
    "buffer_load_dwordx4 v[152:155], %13, %8, %5 offen offset:2048 sc1\n\t" \
    "buffer_load_dwordx4 v[156:159], %13, %8, %5 offen offset:3072 sc1\n\t" \
    "s_add_u32 %3, %3, 1\n\t" \
    "s_cmp_eq_u32 %3, 13\n\t" \
    "s_cselect_b32 %3, 0, %3\n\t" \
    "s_add_u32 %4, %9, %3\n\t" \
    "s_cmp_ge_u32 %4, %23\n\t" \
    "s_cselect_b32 %5, 7, 0\n\t" \
    "s_add_u32 %4, %4, %5\n\t" \
    "s_lshl_b32 %5, %4, 12\n\t" \
    "buffer_load_dwordx4 v[160:163], %13, %8, %5 offen offset:0 sc1\n\t" \
    "buffer_load_dwordx4 v[164:167], %13, %8, %5 offen offset:1024 sc1\n\t" \
    "buffer_load_dwordx4 v[168:171], %13, %8, %5 offen offset:2048 sc1\n\t" \
    "buffer_load_dwordx4 v[172:175], %13, %8, %5 offen offset:3072 sc1\n\t" \
    "s_add_u32 %3, %3, 1\n\t" \
    "s_cmp_eq_u32 %3, 13\n\t" \
    "s_cselect_b32 %3, 0, %3\n\t" \
    "s_setprio 2\n\t" \
    "ds_read_b64_tr_b16 v[192:193], v240 offset:4096\n\t" \
    "ds_read_b64_tr_b16 v[194:195], v240 offset:6144\n\t" \
    "ds_read_b64_tr_b16 v[196:197], v241 offset:4096\n\t" \
    "ds_read_b64_tr_b16 v[198:199], v241 offset:6144\n\t" \
    "ds_read_b64_tr_b16 v[200:201], v242 offset:4096\n\t" \
    "ds_read_b64_tr_b16 v[202:203], v242 offset:6144\n\t" \
    "ds_read_b64_tr_b16 v[204:205], v243 offset:4096\n\t" \
    "ds_read_b64_tr_b16 v[206:207], v243 offset:6144\n\t" \
    "s_waitcnt vmcnt(8)\n\t" \
    "s_waitcnt lgkmcnt(6)\n\t" \
    "v_mfma_f32_16x16x32_bf16 v[64:67], v[192:195], v[128:131], v[64:67]\n\t" \
    "s_add_u32 %4, %9, %3\n\t" \
    "s_cmp_ge_u32 %4, %23\n\t" \
    "s_cselect_b32 %5, 7, 0\n\t" \
    "s_add_u32 %4, %4, %5\n\t" \
    "s_lshl_b32 %5, %4, 12\n\t" \
    "s_add_u32 m0, %11, 0\n\t" \
    "s_lshl_b32 %4, %4, 15\n\t" \
    "buffer_load_dwordx4 %12, %7, %4 offen offset:0 lds\n\t" \
    "v_mfma_f32_16x16x32_bf16 v[68:71], v[192:195], v[132:135], v[68:71]\n\t" \
    "v_mfma_f32_16x16x32_bf16 v[72:75], v[192:195], v[136:139], v[72:75]\n\t" \
    "buffer_load_dwordx4 v249, %7, %4 offen offset:1024 lds\n\t" \
    "v_mfma_f32_16x16x32_bf16 v[76:79], v[192:195], v[140:143], v[76:79]\n\t" \
    "ds_read_b64_tr_b16 v[192:193], v240 offset:38912\n\t" \
    "ds_read_b64_tr_b16 v[194:195], v240 offset:40960\n\t" \
    "s_waitcnt lgkmcnt(6)\n\t" \
    "v_mfma_f32_16x16x32_bf16 v[92:95], v[196:199], v[140:143], v[92:95]\n\t" \
    "buffer_load_dwordx4 v250, %7, %4 offen offset:2048 lds\n\t" \
    "v_mfma_f32_16x16x32_bf16 v[88:91], v[196:199], v[136:139], v[88:91]\n\t" \
    "v_mfma_f32_16x16x32_bf16 v[84:87], v[196:199], v[132:135], v[84:87]\n\t" \
    "buffer_load_dwordx4 v251, %7, %4 offen offset:3072 lds\n\t" \
    "v_mfma_f32_16x16x32_bf16 v[80:83], v[196:199], v[128:131], v[80:83]\n\t" \
    "ds_read_b64_tr_b16 v[196:197], v241 offset:38912\n\t" \
    "ds_read_b64_tr_b16 v[198:199], v241 offset:40960\n\t" \
    "s_waitcnt lgkmcnt(6)\n\t" \
    "v_mfma_f32_16x16x32_bf16 v[96:99], v[200:203], v[128:131], v[96:99]\n\t" \
    "buffer_load_dwordx4 v[176:179], %13, %8, %5 offen offset:0 sc1\n\t" \
    "v_mfma_f32_16x16x32_bf16 v[100:103], v[200:203], v[132:135], v[100:103]\n\t" \
    "v_mfma_f32_16x16x32_bf16 v[104:107], v[200:203], v[136:139], v[104:107]\n\t" \
    "buffer_load_dwordx4 v[180:183], %13, %8, %5 offen offset:1024 sc1\n\t" \
    "v_mfma_f32_16x16x32_bf16 v[108:111], v[200:203], v[140:143], v[108:111]\n\t" \
    "ds_read_b64_tr_b16 v[200:201], v242 offset:38912\n\t" \
    "ds_read_b64_tr_b16 v[202:203], v242 offset:40960\n\t" \
    "s_waitcnt lgkmcnt(6)\n\t" \
    "v_mfma_f32_16x16x32_bf16 v[124:127], v[204:207], v[140:143], v[124:127]\n\t" \
    "buffer_load_dwordx4 v[184:187], %13, %8, %5 offen offset:2048 sc1\n\t" \
    "v_mfma_f32_16x16x32_bf16 v[120:123], v[204:207], v[136:139], v[120:123]\n\t" \
    "v_mfma_f32_16x16x32_bf16 v[116:119], v[204:207], v[132:135], v[116:119]\n\t" \
    "buffer_load_dwordx4 v[188:191], %13, %8, %5 offen offset:3072 sc1\n\t" \
    "s_add_u32 %3, %3, 1\n\t" \
    "s_cmp_eq_u32 %3, 13\n\t" \
    "s_cselect_b32 %3, 0, %3\n\t" \
    "v_mfma_f32_16x16x32_bf16 v[112:115], v[204:207], v[128:131], v[112:115]\n\t" \
    "ds_read_b64_tr_b16 v[204:205], v243 offset:38912\n\t" \
    "ds_read_b64_tr_b16 v[206:207], v243 offset:40960\n\t" \
    "s_waitcnt vmcnt(12)\n\t" \
    "s_waitcnt lgkmcnt(6)\n\t" \
    "v_mfma_f32_16x16x32_bf16 v[64:67], v[192:195], v[144:147], v[64:67]\n\t" \
    "s_add_u32 %4, %9, %3\n\t" \
    "s_cmp_ge_u32 %4, %23\n\t" \
    "s_cselect_b32 %5, 7, 0\n\t" \
    "s_add_u32 %4, %4, %5\n\t" \
    "s_lshl_b32 %5, %4, 12\n\t" \
    "s_add_u32 m0, %11, 4096\n\t" \
    "s_lshl_b32 %4, %4, 15\n\t" \
    "buffer_load_dwordx4 %12, %7, %4 offen offset:0 lds\n\t" \
    "v_mfma_f32_16x16x32_bf16 v[68:71], v[192:195], v[148:151], v[68:71]\n\t" \
    "v_mfma_f32_16x16x32_bf16 v[72:75], v[192:195], v[152:155], v[72:75]\n\t" \
    "buffer_load_dwordx4 v249, %7, %4 offen offset:1024 lds\n\t" \
    "v_mfma_f32_16x16x32_bf16 v[76:79], v[192:195], v[156:159], v[76:79]\n\t" \
    "ds_read_b64_tr_b16 v[192:193], v240 offset:43008\n\t" \
    "ds_read_b64_tr_b16 v[194:195], v240 offset:45056\n\t" \
    "s_waitcnt lgkmcnt(6)\n\t" \
    "v_mfma_f32_16x16x32_bf16 v[92:95], v[196:199], v[156:159], v[92:95]\n\t" \
    "buffer_load_dwordx4 v250, %7, %4 offen offset:2048 lds\n\t" \
    "v_mfma_f32_16x16x32_bf16 v[88:91], v[196:199], v[152:155], v[88:91]\n\t" \
    "v_mfma_f32_16x16x32_bf16 v[84:87], v[196:199], v[148:151], v[84:87]\n\t" \
    "buffer_load_dwordx4 v251, %7, %4 offen offset:3072 lds\n\t" \
    "v_mfma_f32_16x16x32_bf16 v[80:83], v[196:199], v[144:147], v[80:83]\n\t" \
    "ds_read_b64_tr_b16 v[196:197], v241 offset:43008\n\t" \
    "ds_read_b64_tr_b16 v[198:199], v241 offset:45056\n\t" \
    "s_waitcnt lgkmcnt(6)\n\t" \
    "v_mfma_f32_16x16x32_bf16 v[96:99], v[200:203], v[144:147], v[96:99]\n\t" \
    "buffer_load_dwordx4 v[128:131], %13, %8, %5 offen offset:0 sc1\n\t" \
    "v_mfma_f32_16x16x32_bf16 v[100:103], v[200:203], v[148:151], v[100:103]\n\t" \
    "v_mfma_f32_16x16x32_bf16 v[104:107], v[200:203], v[152:155], v[104:107]\n\t" \
    "buffer_load_dwordx4 v[132:135], %13, %8, %5 offen offset:1024 sc1\n\t" \
    "v_mfma_f32_16x16x32_bf16 v[108:111], v[200:203], v[156:159], v[108:111]\n\t" \
    "ds_read_b64_tr_b16 v[200:201], v242 offset:43008\n\t" \
    "ds_read_b64_tr_b16 v[202:203], v242 offset:45056\n\t" \
    "s_waitcnt lgkmcnt(6)\n\t" \
    "v_mfma_f32_16x16x32_bf16 v[124:127], v[204:207], v[156:159], v[124:127]\n\t" \
    "buffer_load_dwordx4 v[136:139], %13, %8, %5 offen offset:2048 sc1\n\t" \
    "v_mfma_f32_16x16x32_bf16 v[120:123], v[204:207], v[152:155], v[120:123]\n\t" \
    "v_mfma_f32_16x16x32_bf16 v[116:119], v[204:207], v[148:151], v[116:119]\n\t" \
    "buffer_load_dwordx4 v[140:143], %13, %8, %5 offen offset:3072 sc1\n\t" \
    "s_add_u32 %3, %3, 1\n\t" \
    "s_cmp_eq_u32 %3, 13\n\t" \
    "s_cselect_b32 %3, 0, %3\n\t" \
    "v_mfma_f32_16x16x32_bf16 v[112:115], v[204:207], v[144:147], v[112:115]\n\t" \
    "ds_read_b64_tr_b16 v[204:205], v243 offset:43008\n\t" \
    "ds_read_b64_tr_b16 v[206:207], v243 offset:45056\n\t" \
    "s_waitcnt vmcnt(12)\n\t" \
    "s_waitcnt lgkmcnt(6)\n\t" \
    "v_mfma_f32_16x16x32_bf16 v[64:67], v[192:195], v[160:163], v[64:67]\n\t" \
    "s_add_u32 %4, %9, %3\n\t" \
    "s_cmp_ge_u32 %4, %23\n\t" \
    "s_cselect_b32 %5, 7, 0\n\t" \
    "s_add_u32 %4, %4, %5\n\t" \
    "s_lshl_b32 %5, %4, 12\n\t" \
    "s_add_u32 m0, %10, 0\n\t" \
    "s_lshl_b32 %4, %4, 15\n\t" \
    "buffer_load_dwordx4 %12, %7, %4 offen offset:0 lds\n\t" \
    "v_mfma_f32_16x16x32_bf16 v[68:71], v[192:195], v[164:167], v[68:71]\n\t" \
    "v_mfma_f32_16x16x32_bf16 v[72:75], v[192:195], v[168:171], v[72:75]\n\t" \
    "buffer_load_dwordx4 v249, %7, %4 offen offset:1024 lds\n\t" \
    "v_mfma_f32_16x16x32_bf16 v[76:79], v[192:195], v[172:175], v[76:79]\n\t" \
    "ds_read_b64_tr_b16 v[192:193], v240 offset:0\n\t" \
    "ds_read_b64_tr_b16 v[194:195], v240 offset:2048\n\t" \
    "s_waitcnt lgkmcnt(6)\n\t" \
    "v_mfma_f32_16x16x32_bf16 v[92:95], v[196:199], v[172:175], v[92:95]\n\t" \
    "buffer_load_dwordx4 v250, %7, %4 offen offset:2048 lds\n\t" \
    "v_mfma_f32_16x16x32_bf16 v[88:91], v[196:199], v[168:171], v[88:91]\n\t" \
    "v_mfma_f32_16x16x32_bf16 v[84:87], v[196:199], v[164:167], v[84:87]\n\t" \
    "buffer_load_dwordx4 v251, %7, %4 offen offset:3072 lds\n\t" \
    "v_mfma_f32_16x16x32_bf16 v[80:83], v[196:199], v[160:163], v[80:83]\n\t" \
    "ds_read_b64_tr_b16 v[196:197], v241 offset:0\n\t" \
    "ds_read_b64_tr_b16 v[198:199], v241 offset:2048\n\t" \
    "s_waitcnt lgkmcnt(6)\n\t" \
    "v_mfma_f32_16x16x32_bf16 v[96:99], v[200:203], v[160:163], v[96:99]\n\t" \
    "buffer_load_dwordx4 v[144:147], %13, %8, %5 offen offset:0 sc1\n\t" \
    "v_mfma_f32_16x16x32_bf16 v[100:103], v[200:203], v[164:167], v[100:103]\n\t" \
    "v_mfma_f32_16x16x32_bf16 v[104:107], v[200:203], v[168:171], v[104:107]\n\t" \
    "buffer_load_dwordx4 v[148:151], %13, %8, %5 offen offset:1024 sc1\n\t" \
    "v_mfma_f32_16x16x32_bf16 v[108:111], v[200:203], v[172:175], v[108:111]\n\t" \
    "ds_read_b64_tr_b16 v[200:201], v242 offset:0\n\t" \
    "ds_read_b64_tr_b16 v[202:203], v242 offset:2048\n\t" \
    "s_waitcnt lgkmcnt(6)\n\t" \
    "v_mfma_f32_16x16x32_bf16 v[124:127], v[204:207], v[172:175], v[124:127]\n\t" \
    "buffer_load_dwordx4 v[152:155], %13, %8, %5 offen offset:2048 sc1\n\t" \
    "v_mfma_f32_16x16x32_bf16 v[120:123], v[204:207], v[168:171], v[120:123]\n\t" \
    "v_mfma_f32_16x16x32_bf16 v[116:119], v[204:207], v[164:167], v[116:119]\n\t" \
    "buffer_load_dwordx4 v[156:159], %13, %8, %5 offen offset:3072 sc1\n\t" \
    "s_add_u32 %3, %3, 1\n\t" \
    "s_cmp_eq_u32 %3, 13\n\t" \
    "s_cselect_b32 %3, 0, %3\n\t" \
    "v_mfma_f32_16x16x32_bf16 v[112:115], v[204:207], v[160:163], v[112:115]\n\t" \
    "ds_read_b64_tr_b16 v[204:205], v243 offset:0\n\t" \
    "ds_read_b64_tr_b16 v[206:207], v243 offset:2048\n\t" \
    "s_waitcnt vmcnt(12)\n\t" \
    "s_waitcnt lgkmcnt(6)\n\t" \
    "v_mfma_f32_16x16x32_bf16 v[64:67], v[192:195], v[176:179], v[64:67]\n\t" \
    "s_add_u32 %4, %9, %3\n\t" \
    "s_cmp_ge_u32 %4, %23\n\t" \
    "s_cselect_b32 %5, 7, 0\n\t" \
    "s_add_u32 %4, %4, %5\n\t" \
    "s_lshl_b32 %5, %4, 12\n\t" \
    "s_add_u32 m0, %10, 4096\n\t" \
    "s_lshl_b32 %4, %4, 15\n\t" \
    "buffer_load_dwordx4 %12, %7, %4 offen offset:0 lds\n\t" \
    "v_mfma_f32_16x16x32_bf16 v[68:71], v[192:195], v[180:183], v[68:71]\n\t" \
    "v_mfma_f32_16x16x32_bf16 v[72:75], v[192:195], v[184:187], v[72:75]\n\t" \
    "buffer_load_dwordx4 v249, %7, %4 offen offset:1024 lds\n\t" \
    "v_mfma_f32_16x16x32_bf16 v[76:79], v[192:195], v[188:191], v[76:79]\n\t" \
    "ds_read_b64_tr_b16 v[192:193], v240 offset:4096\n\t" \
    "ds_read_b64_tr_b16 v[194:195], v240 offset:6144\n\t" \
    "s_waitcnt lgkmcnt(6)\n\t" \
    "v_mfma_f32_16x16x32_bf16 v[92:95], v[196:199], v[188:191], v[92:95]\n\t" \
    "buffer_load_dwordx4 v250, %7, %4 offen offset:2048 lds\n\t" \
    "v_mfma_f32_16x16x32_bf16 v[88:91], v[196:199], v[184:187], v[88:91]\n\t" \
    "v_mfma_f32_16x16x32_bf16 v[84:87], v[196:199], v[180:183], v[84:87]\n\t" \
    "buffer_load_dwordx4 v251, %7, %4 offen offset:3072 lds\n\t" \
    "v_mfma_f32_16x16x32_bf16 v[80:83], v[196:199], v[176:179], v[80:83]\n\t" \
    "ds_read_b64_tr_b16 v[196:197], v241 offset:4096\n\t" \
    "ds_read_b64_tr_b16 v[198:199], v241 offset:6144\n\t" \
    "s_waitcnt lgkmcnt(6)\n\t" \
    "v_mfma_f32_16x16x32_bf16 v[96:99], v[200:203], v[176:179], v[96:99]\n\t" \
    "buffer_load_dwordx4 v[160:163], %13, %8, %5 offen offset:0 sc1\n\t" \
    "v_mfma_f32_16x16x32_bf16 v[100:103], v[200:203], v[180:183], v[100:103]\n\t" \
    "v_mfma_f32_16x16x32_bf16 v[104:107], v[200:203], v[184:187], v[104:107]\n\t" \
    "buffer_load_dwordx4 v[164:167], %13, %8, %5 offen offset:1024 sc1\n\t" \
    "v_mfma_f32_16x16x32_bf16 v[108:111], v[200:203], v[188:191], v[108:111]\n\t" \
    "ds_read_b64_tr_b16 v[200:201], v242 offset:4096\n\t" \
    "ds_read_b64_tr_b16 v[202:203], v242 offset:6144\n\t" \
    "s_waitcnt lgkmcnt(6)\n\t" \
    "v_mfma_f32_16x16x32_bf16 v[124:127], v[204:207], v[188:191], v[124:127]\n\t" \
    "buffer_load_dwordx4 v[168:171], %13, %8, %5 offen offset:2048 sc1\n\t" \
    "v_mfma_f32_16x16x32_bf16 v[120:123], v[204:207], v[184:187], v[120:123]\n\t" \
    "v_mfma_f32_16x16x32_bf16 v[116:119], v[204:207], v[180:183], v[116:119]\n\t" \
    "buffer_load_dwordx4 v[172:175], %13, %8, %5 offen offset:3072 sc1\n\t" \
    "s_add_u32 %3, %3, 1\n\t" \
    "s_cmp_eq_u32 %3, 13\n\t" \
    "s_cselect_b32 %3, 0, %3\n\t" \
    "v_mfma_f32_16x16x32_bf16 v[112:115], v[204:207], v[176:179], v[112:115]\n\t" \
    "ds_read_b64_tr_b16 v[204:205], v243 offset:4096\n\t" \
    "ds_read_b64_tr_b16 v[206:207], v243 offset:6144\n\t" \
    "s_waitcnt vmcnt(12)\n\t" \
    "s_waitcnt lgkmcnt(6)\n\t" \
    "v_mfma_f32_16x16x32_bf16 v[64:67], v[192:195], v[128:131], v[64:67]\n\t" \
    "s_add_u32 %4, %9, %3\n\t" \
    "s_cmp_ge_u32 %4, %23\n\t" \
    "s_cselect_b32 %5, 7, 0\n\t" \
    "s_add_u32 %4, %4, %5\n\t" \
    "s_lshl_b32 %5, %4, 12\n\t" \
    "s_add_u32 m0, %11, 0\n\t" \
    "s_lshl_b32 %4, %4, 15\n\t" \
    "buffer_load_dwordx4 %12, %7, %4 offen offset:0 lds\n\t" \
    "v_mfma_f32_16x16x32_bf16 v[68:71], v[192:195], v[132:135], v[68:71]\n\t" \
    "v_mfma_f32_16x16x32_bf16 v[72:75], v[192:195], v[136:139], v[72:75]\n\t" \
    "buffer_load_dwordx4 v249, %7, %4 offen offset:1024 lds\n\t" \
    "v_mfma_f32_16x16x32_bf16 v[76:79], v[192:195], v[140:143], v[76:79]\n\t" \
    "ds_read_b64_tr_b16 v[192:193], v240 offset:38912\n\t" \
    "ds_read_b64_tr_b16 v[194:195], v240 offset:40960\n\t" \
    "s_waitcnt lgkmcnt(6)\n\t" \
    "v_mfma_f32_16x16x32_bf16 v[92:95], v[196:199], v[140:143], v[92:95]\n\t" \
    "buffer_load_dwordx4 v250, %7, %4 offen offset:2048 lds\n\t" \
    "v_mfma_f32_16x16x32_bf16 v[88:91], v[196:199], v[136:139], v[88:91]\n\t" \
    "v_mfma_f32_16x16x32_bf16 v[84:87], v[196:199], v[132:135], v[84:87]\n\t" \
    "buffer_load_dwordx4 v251, %7, %4 offen offset:3072 lds\n\t" \
    "v_mfma_f32_16x16x32_bf16 v[80:83], v[196:199], v[128:131], v[80:83]\n\t" \
    "ds_read_b64_tr_b16 v[196:197], v241 offset:38912\n\t" \
    "ds_read_b64_tr_b16 v[198:199], v241 offset:40960\n\t" \
    "s_waitcnt lgkmcnt(6)\n\t" \
    "v_mfma_f32_16x16x32_bf16 v[96:99], v[200:203], v[128:131], v[96:99]\n\t" \
    "buffer_load_dwordx4 v[176:179], %13, %8, %5 offen offset:0 sc1\n\t" \
    "v_mfma_f32_16x16x32_bf16 v[100:103], v[200:203], v[132:135], v[100:103]\n\t" \
    "v_mfma_f32_16x16x32_bf16 v[104:107], v[200:203], v[136:139], v[104:107]\n\t" \
    "buffer_load_dwordx4 v[180:183], %13, %8, %5 offen offset:1024 sc1\n\t" \
    "v_mfma_f32_16x16x32_bf16 v[108:111], v[200:203], v[140:143], v[108:111]\n\t" \
    "ds_read_b64_tr_b16 v[200:201], v242 offset:38912\n\t" \
    "ds_read_b64_tr_b16 v[202:203], v242 offset:40960\n\t" \
    "s_waitcnt lgkmcnt(6)\n\t" \
    "v_mfma_f32_16x16x32_bf16 v[124:127], v[204:207], v[140:143], v[124:127]\n\t" \
    "buffer_load_dwordx4 v[184:187], %13, %8, %5 offen offset:2048 sc1\n\t" \
    "v_mfma_f32_16x16x32_bf16 v[120:123], v[204:207], v[136:139], v[120:123]\n\t" \
    "v_mfma_f32_16x16x32_bf16 v[116:119], v[204:207], v[132:135], v[116:119]\n\t" \
    "buffer_load_dwordx4 v[188:191], %13, %8, %5 offen offset:3072 sc1\n\t" \
    "s_add_u32 %3, %3, 1\n\t" \
    "s_cmp_eq_u32 %3, 13\n\t" \
    "s_cselect_b32 %3, 0, %3\n\t" \
    "v_mfma_f32_16x16x32_bf16 v[112:115], v[204:207], v[128:131], v[112:115]\n\t" \
    "ds_read_b64_tr_b16 v[204:205], v243 offset:38912\n\t" \
    "ds_read_b64_tr_b16 v[206:207], v243 offset:40960\n\t" \
    "s_waitcnt vmcnt(12)\n\t" \
    "s_waitcnt lgkmcnt(6)\n\t" \
    "v_mfma_f32_16x16x32_bf16 v[64:67], v[192:195], v[144:147], v[64:67]\n\t" \
    "s_add_u32 %4, %9, %3\n\t" \
    "s_cmp_ge_u32 %4, %23\n\t" \
    "s_cselect_b32 %5, 7, 0\n\t" \
    "s_add_u32 %4, %4, %5\n\t" \
    "s_lshl_b32 %5, %4, 12\n\t" \
    "s_add_u32 m0, %11, 4096\n\t" \
    "s_lshl_b32 %4, %4, 15\n\t" \
    "buffer_load_dwordx4 %12, %7, %4 offen offset:0 lds\n\t" \
    "v_mfma_f32_16x16x32_bf16 v[68:71], v[192:195], v[148:151], v[68:71]\n\t" \
    "v_mfma_f32_16x16x32_bf16 v[72:75], v[192:195], v[152:155], v[72:75]\n\t" \
    "buffer_load_dwordx4 v249, %7, %4 offen offset:1024 lds\n\t" \
    "v_mfma_f32_16x16x32_bf16 v[76:79], v[192:195], v[156:159], v[76:79]\n\t" \
    "ds_read_b64_tr_b16 v[192:193], v240 offset:43008\n\t" \
    "ds_read_b64_tr_b16 v[194:195], v240 offset:45056\n\t" \
    "s_waitcnt lgkmcnt(6)\n\t" \
    "v_mfma_f32_16x16x32_bf16 v[92:95], v[196:199], v[156:159], v[92:95]\n\t" \
    "buffer_load_dwordx4 v250, %7, %4 offen offset:2048 lds\n\t" \
    "v_mfma_f32_16x16x32_bf16 v[88:91], v[196:199], v[152:155], v[88:91]\n\t" \
    "v_mfma_f32_16x16x32_bf16 v[84:87], v[196:199], v[148:151], v[84:87]\n\t" \
    "buffer_load_dwordx4 v251, %7, %4 offen offset:3072 lds\n\t" \
    "v_mfma_f32_16x16x32_bf16 v[80:83], v[196:199], v[144:147], v[80:83]\n\t" \
    "ds_read_b64_tr_b16 v[196:197], v241 offset:43008\n\t" \
    "ds_read_b64_tr_b16 v[198:199], v241 offset:45056\n\t" \
    "s_waitcnt lgkmcnt(6)\n\t" \
    "v_mfma_f32_16x16x32_bf16 v[96:99], v[200:203], v[144:147], v[96:99]\n\t" \
    "buffer_load_dwordx4 v[128:131], %13, %8, %5 offen offset:0 sc1\n\t" \
    "v_mfma_f32_16x16x32_bf16 v[100:103], v[200:203], v[148:151], v[100:103]\n\t" \
    "v_mfma_f32_16x16x32_bf16 v[104:107], v[200:203], v[152:155], v[104:107]\n\t" \
    "buffer_load_dwordx4 v[132:135], %13, %8, %5 offen offset:1024 sc1\n\t" \
    "v_mfma_f32_16x16x32_bf16 v[108:111], v[200:203], v[156:159], v[108:111]\n\t" \
    "ds_read_b64_tr_b16 v[200:201], v242 offset:43008\n\t" \
    "ds_read_b64_tr_b16 v[202:203], v242 offset:45056\n\t" \
    "s_waitcnt lgkmcnt(6)\n\t" \
    "v_mfma_f32_16x16x32_bf16 v[124:127], v[204:207], v[156:159], v[124:127]\n\t" \
    "buffer_load_dwordx4 v[136:139], %13, %8, %5 offen offset:2048 sc1\n\t" \
    "v_mfma_f32_16x16x32_bf16 v[120:123], v[204:207], v[152:155], v[120:123]\n\t" \
    "v_mfma_f32_16x16x32_bf16 v[116:119], v[204:207], v[148:151], v[116:119]\n\t" \
    "buffer_load_dwordx4 v[140:143], %13, %8, %5 offen offset:3072 sc1\n\t" \
    "s_add_u32 %3, %3, 1\n\t" \
    "s_cmp_eq_u32 %3, 13\n\t" \
    "s_cselect_b32 %3, 0, %3\n\t" \
    "v_mfma_f32_16x16x32_bf16 v[112:115], v[204:207], v[144:147], v[112:115]\n\t" \
    "ds_read_b64_tr_b16 v[204:205], v243 offset:43008\n\t" \
    "ds_read_b64_tr_b16 v[206:207], v243 offset:45056\n\t" \
    "s_waitcnt vmcnt(12)\n\t" \
    "s_waitcnt lgkmcnt(6)\n\t" \
    "v_mfma_f32_16x16x32_bf16 v[64:67], v[192:195], v[160:163], v[64:67]\n\t" \
    "s_add_u32 %4, %9, %3\n\t" \
    "s_cmp_ge_u32 %4, %23\n\t" \
    "s_cselect_b32 %5, 7, 0\n\t" \
    "s_add_u32 %4, %4, %5\n\t" \
    "s_lshl_b32 %5, %4, 12\n\t" \
    "s_add_u32 m0, %10, 0\n\t" \
    "s_lshl_b32 %4, %4, 15\n\t" \
    "buffer_load_dwordx4 %12, %7, %4 offen offset:0 lds\n\t" \
    "v_mfma_f32_16x16x32_bf16 v[68:71], v[192:195], v[164:167], v[68:71]\n\t" \
    "v_mfma_f32_16x16x32_bf16 v[72:75], v[192:195], v[168:171], v[72:75]\n\t" \
    "buffer_load_dwordx4 v249, %7, %4 offen offset:1024 lds\n\t" \
    "v_mfma_f32_16x16x32_bf16 v[76:79], v[192:195], v[172:175], v[76:79]\n\t" \
    "ds_read_b64_tr_b16 v[192:193], v240 offset:0\n\t" \
    "ds_read_b64_tr_b16 v[194:195], v240 offset:2048\n\t" \
    "s_waitcnt lgkmcnt(6)\n\t" \
    "v_mfma_f32_16x16x32_bf16 v[92:95], v[196:199], v[172:175], v[92:95]\n\t" \
    "buffer_load_dwordx4 v250, %7, %4 offen offset:2048 lds\n\t" \
    "v_mfma_f32_16x16x32_bf16 v[88:91], v[196:199], v[168:171], v[88:91]\n\t" \
    "v_mfma_f32_16x16x32_bf16 v[84:87], v[196:199], v[164:167], v[84:87]\n\t" \
    "buffer_load_dwordx4 v251, %7, %4 offen offset:3072 lds\n\t" \
    "v_mfma_f32_16x16x32_bf16 v[80:83], v[196:199], v[160:163], v[80:83]\n\t" \
    "ds_read_b64_tr_b16 v[196:197], v241 offset:0\n\t" \
    "ds_read_b64_tr_b16 v[198:199], v241 offset:2048\n\t" \
    "s_waitcnt lgkmcnt(6)\n\t" \
    "v_mfma_f32_16x16x32_bf16 v[96:99], v[200:203], v[160:163], v[96:99]\n\t" \
    "buffer_load_dwordx4 v[144:147], %13, %8, %5 offen offset:0 sc1\n\t" \
    "v_mfma_f32_16x16x32_bf16 v[100:103], v[200:203], v[164:167], v[100:103]\n\t" \
    "v_mfma_f32_16x16x32_bf16 v[104:107], v[200:203], v[168:171], v[104:107]\n\t" \
    "buffer_load_dwordx4 v[148:151], %13, %8, %5 offen offset:1024 sc1\n\t" \
    "v_mfma_f32_16x16x32_bf16 v[108:111], v[200:203], v[172:175], v[108:111]\n\t" \
    "ds_read_b64_tr_b16 v[200:201], v242 offset:0\n\t" \
    "ds_read_b64_tr_b16 v[202:203], v242 offset:2048\n\t" \
    "s_waitcnt lgkmcnt(6)\n\t" \
    "v_mfma_f32_16x16x32_bf16 v[124:127], v[204:207], v[172:175], v[124:127]\n\t" \
    "buffer_load_dwordx4 v[152:155], %13, %8, %5 offen offset:2048 sc1\n\t" \
    "v_mfma_f32_16x16x32_bf16 v[120:123], v[204:207], v[168:171], v[120:123]\n\t" \
    "v_mfma_f32_16x16x32_bf16 v[116:119], v[204:207], v[164:167], v[116:119]\n\t" \
    "buffer_load_dwordx4 v[156:159], %13, %8, %5 offen offset:3072 sc1\n\t" \
    "s_add_u32 %3, %3, 1\n\t" \
    "s_cmp_eq_u32 %3, 13\n\t" \
    "s_cselect_b32 %3, 0, %3\n\t" \
    "v_mfma_f32_16x16x32_bf16 v[112:115], v[204:207], v[160:163], v[112:115]\n\t" \
    "ds_read_b64_tr_b16 v[204:205], v243 offset:0\n\t" \
    "ds_read_b64_tr_b16 v[206:207], v243 offset:2048\n\t" \
    "s_waitcnt vmcnt(12)\n\t" \
    "s_waitcnt lgkmcnt(6)\n\t" \
    "v_mfma_f32_16x16x32_bf16 v[64:67], v[192:195], v[176:179], v[64:67]\n\t" \
    "s_add_u32 %4, %9, %3\n\t" \
    "s_cmp_ge_u32 %4, %23\n\t" \
    "s_cselect_b32 %5, 7, 0\n\t" \
    "s_add_u32 %4, %4, %5\n\t" \
    "s_lshl_b32 %5, %4, 12\n\t" \
    "s_add_u32 m0, %10, 4096\n\t" \
    "s_lshl_b32 %4, %4, 15\n\t" \
    "buffer_load_dwordx4 %12, %7, %4 offen offset:0 lds\n\t" \
    "v_mfma_f32_16x16x32_bf16 v[68:71], v[192:195], v[180:183], v[68:71]\n\t" \
    "v_mfma_f32_16x16x32_bf16 v[72:75], v[192:195], v[184:187], v[72:75]\n\t" \
    "buffer_load_dwordx4 v249, %7, %4 offen offset:1024 lds\n\t" \
    "v_mfma_f32_16x16x32_bf16 v[76:79], v[192:195], v[188:191], v[76:79]\n\t" \
    "ds_read_b64_tr_b16 v[192:193], v240 offset:4096\n\t" \
    "ds_read_b64_tr_b16 v[194:195], v240 offset:6144\n\t" \
    "s_waitcnt lgkmcnt(6)\n\t" \
    "v_mfma_f32_16x16x32_bf16 v[92:95], v[196:199], v[188:191], v[92:95]\n\t" \
    "buffer_load_dwordx4 v250, %7, %4 offen offset:2048 lds\n\t" \
    "v_mfma_f32_16x16x32_bf16 v[88:91], v[196:199], v[184:187], v[88:91]\n\t" \
    "v_mfma_f32_16x16x32_bf16 v[84:87], v[196:199], v[180:183], v[84:87]\n\t" \
    "buffer_load_dwordx4 v251, %7, %4 offen offset:3072 lds\n\t" \
    "v_mfma_f32_16x16x32_bf16 v[80:83], v[196:199], v[176:179], v[80:83]\n\t" \
    "ds_read_b64_tr_b16 v[196:197], v241 offset:4096\n\t" \
    "ds_read_b64_tr_b16 v[198:199], v241 offset:6144\n\t" \
    "s_waitcnt lgkmcnt(6)\n\t" \
    "v_mfma_f32_16x16x32_bf16 v[96:99], v[200:203], v[176:179], v[96:99]\n\t" \
    "buffer_load_dwordx4 v[160:163], %13, %8, %5 offen offset:0 sc1\n\t" \
    "v_mfma_f32_16x16x32_bf16 v[100:103], v[200:203], v[180:183], v[100:103]\n\t" \
    "v_mfma_f32_16x16x32_bf16 v[104:107], v[200:203], v[184:187], v[104:107]\n\t" \
    "buffer_load_dwordx4 v[164:167], %13, %8, %5 offen offset:1024 sc1\n\t" \
    "v_mfma_f32_16x16x32_bf16 v[108:111], v[200:203], v[188:191], v[108:111]\n\t" \
    "ds_read_b64_tr_b16 v[200:201], v242 offset:4096\n\t" \
    "ds_read_b64_tr_b16 v[202:203], v242 offset:6144\n\t" \
    "s_waitcnt lgkmcnt(6)\n\t" \
    "v_mfma_f32_16x16x32_bf16 v[124:127], v[204:207], v[188:191], v[124:127]\n\t" \
    "buffer_load_dwordx4 v[168:171], %13, %8, %5 offen offset:2048 sc1\n\t" \
    "v_mfma_f32_16x16x32_bf16 v[120:123], v[204:207], v[184:187], v[120:123]\n\t" \
    "v_mfma_f32_16x16x32_bf16 v[116:119], v[204:207], v[180:183], v[116:119]\n\t" \
    "buffer_load_dwordx4 v[172:175], %13, %8, %5 offen offset:3072 sc1\n\t" \
    "s_add_u32 %3, %3, 1\n\t" \
    "s_cmp_eq_u32 %3, 13\n\t" \
    "s_cselect_b32 %3, 0, %3\n\t" \
    "v_mfma_f32_16x16x32_bf16 v[112:115], v[204:207], v[176:179], v[112:115]\n\t" \
    "ds_read_b64_tr_b16 v[204:205], v243 offset:4096\n\t" \
    "ds_read_b64_tr_b16 v[206:207], v243 offset:6144\n\t" \
    "s_waitcnt vmcnt(12)\n\t" \
    "s_waitcnt lgkmcnt(6)\n\t" \
    "v_mfma_f32_16x16x32_bf16 v[64:67], v[192:195], v[128:131], v[64:67]\n\t" \
    "s_add_u32 %4, %9, %3\n\t" \
    "s_cmp_ge_u32 %4, %23\n\t" \
    "s_cselect_b32 %5, 7, 0\n\t" \
    "s_add_u32 %4, %4, %5\n\t" \
    "s_lshl_b32 %5, %4, 12\n\t" \
    "s_add_u32 m0, %11, 0\n\t" \
    "s_lshl_b32 %4, %4, 15\n\t" \
    "buffer_load_dwordx4 %12, %7, %4 offen offset:0 lds\n\t" \
    "v_mfma_f32_16x16x32_bf16 v[68:71], v[192:195], v[132:135], v[68:71]\n\t" \
    "v_mfma_f32_16x16x32_bf16 v[72:75], v[192:195], v[136:139], v[72:75]\n\t" \
    "buffer_load_dwordx4 v249, %7, %4 offen offset:1024 lds\n\t" \
    "v_mfma_f32_16x16x32_bf16 v[76:79], v[192:195], v[140:143], v[76:79]\n\t" \
    "ds_read_b64_tr_b16 v[192:193], v240 offset:38912\n\t" \
    "ds_read_b64_tr_b16 v[194:195], v240 offset:40960\n\t" \
    "s_waitcnt lgkmcnt(6)\n\t" \
    "v_mfma_f32_16x16x32_bf16 v[92:95], v[196:199], v[140:143], v[92:95]\n\t" \
    "buffer_load_dwordx4 v250, %7, %4 offen offset:2048 lds\n\t" \
    "v_mfma_f32_16x16x32_bf16 v[88:91], v[196:199], v[136:139], v[88:91]\n\t" \
    "v_mfma_f32_16x16x32_bf16 v[84:87], v[196:199], v[132:135], v[84:87]\n\t" \
    "buffer_load_dwordx4 v251, %7, %4 offen offset:3072 lds\n\t" \
    "v_mfma_f32_16x16x32_bf16 v[80:83], v[196:199], v[128:131], v[80:83]\n\t" \
    "ds_read_b64_tr_b16 v[196:197], v241 offset:38912\n\t" \
    "ds_read_b64_tr_b16 v[198:199], v241 offset:40960\n\t" \
    "s_waitcnt lgkmcnt(6)\n\t" \
    "v_mfma_f32_16x16x32_bf16 v[96:99], v[200:203], v[128:131], v[96:99]\n\t" \
    "buffer_load_dwordx4 v[176:179], %13, %8, %5 offen offset:0 sc1\n\t" \
    "v_mfma_f32_16x16x32_bf16 v[100:103], v[200:203], v[132:135], v[100:103]\n\t" \
    "v_mfma_f32_16x16x32_bf16 v[104:107], v[200:203], v[136:139], v[104:107]\n\t" \
    "buffer_load_dwordx4 v[180:183], %13, %8, %5 offen offset:1024 sc1\n\t" \
    "v_mfma_f32_16x16x32_bf16 v[108:111], v[200:203], v[140:143], v[108:111]\n\t" \
    "ds_read_b64_tr_b16 v[200:201], v242 offset:38912\n\t" \
    "ds_read_b64_tr_b16 v[202:203], v242 offset:40960\n\t" \
    "s_waitcnt lgkmcnt(6)\n\t" \
    "v_mfma_f32_16x16x32_bf16 v[124:127], v[204:207], v[140:143], v[124:127]\n\t" \
    "buffer_load_dwordx4 v[184:187], %13, %8, %5 offen offset:2048 sc1\n\t" \
    "v_mfma_f32_16x16x32_bf16 v[120:123], v[204:207], v[136:139], v[120:123]\n\t" \
    "v_mfma_f32_16x16x32_bf16 v[116:119], v[204:207], v[132:135], v[116:119]\n\t" \
    "buffer_load_dwordx4 v[188:191], %13, %8, %5 offen offset:3072 sc1\n\t" \
    "s_add_u32 %3, %3, 1\n\t" \
    "s_cmp_eq_u32 %3, 13\n\t" \
    "s_cselect_b32 %3, 0, %3\n\t" \
    "v_mfma_f32_16x16x32_bf16 v[112:115], v[204:207], v[128:131], v[112:115]\n\t" \
    "ds_read_b64_tr_b16 v[204:205], v243 offset:38912\n\t" \
    "ds_read_b64_tr_b16 v[206:207], v243 offset:40960\n\t" \
    "s_waitcnt vmcnt(12)\n\t" \
    "s_waitcnt lgkmcnt(6)\n\t" \
    "v_mfma_f32_16x16x32_bf16 v[64:67], v[192:195], v[144:147], v[64:67]\n\t" \
    "s_add_u32 %4, %9, %3\n\t" \
    "s_cmp_ge_u32 %4, %23\n\t" \
    "s_cselect_b32 %5, 7, 0\n\t" \
    "s_add_u32 %4, %4, %5\n\t" \
    "s_lshl_b32 %5, %4, 12\n\t" \
    "s_add_u32 m0, %11, 4096\n\t" \
    "s_lshl_b32 %4, %4, 15\n\t" \
    "buffer_load_dwordx4 %12, %7, %4 offen offset:0 lds\n\t" \
    "v_mfma_f32_16x16x32_bf16 v[68:71], v[192:195], v[148:151], v[68:71]\n\t" \
    "v_mfma_f32_16x16x32_bf16 v[72:75], v[192:195], v[152:155], v[72:75]\n\t" \
    "buffer_load_dwordx4 v249, %7, %4 offen offset:1024 lds\n\t" \
    "v_mfma_f32_16x16x32_bf16 v[76:79], v[192:195], v[156:159], v[76:79]\n\t" \
    "ds_read_b64_tr_b16 v[192:193], v240 offset:43008\n\t" \
    "ds_read_b64_tr_b16 v[194:195], v240 offset:45056\n\t" \
    "s_waitcnt lgkmcnt(6)\n\t" \
    "v_mfma_f32_16x16x32_bf16 v[92:95], v[196:199], v[156:159], v[92:95]\n\t" \
    "buffer_load_dwordx4 v250, %7, %4 offen offset:2048 lds\n\t" \
    "v_mfma_f32_16x16x32_bf16 v[88:91], v[196:199], v[152:155], v[88:91]\n\t" \
    "v_mfma_f32_16x16x32_bf16 v[84:87], v[196:199], v[148:151], v[84:87]\n\t" \
    "buffer_load_dwordx4 v251, %7, %4 offen offset:3072 lds\n\t" \
    "v_mfma_f32_16x16x32_bf16 v[80:83], v[196:199], v[144:147], v[80:83]\n\t" \
    "ds_read_b64_tr_b16 v[196:197], v241 offset:43008\n\t" \
    "ds_read_b64_tr_b16 v[198:199], v241 offset:45056\n\t" \
    "s_waitcnt lgkmcnt(6)\n\t" \
    "v_mfma_f32_16x16x32_bf16 v[96:99], v[200:203], v[144:147], v[96:99]\n\t" \
    "buffer_load_dwordx4 v[128:131], %13, %8, %5 offen offset:0 sc1\n\t" \
    "v_mfma_f32_16x16x32_bf16 v[100:103], v[200:203], v[148:151], v[100:103]\n\t" \
    "v_mfma_f32_16x16x32_bf16 v[104:107], v[200:203], v[152:155], v[104:107]\n\t" \
    "buffer_load_dwordx4 v[132:135], %13, %8, %5 offen offset:1024 sc1\n\t" \
    "v_mfma_f32_16x16x32_bf16 v[108:111], v[200:203], v[156:159], v[108:111]\n\t" \
    "ds_read_b64_tr_b16 v[200:201], v242 offset:43008\n\t" \
    "ds_read_b64_tr_b16 v[202:203], v242 offset:45056\n\t" \
    "s_waitcnt lgkmcnt(6)\n\t" \
    "v_mfma_f32_16x16x32_bf16 v[124:127], v[204:207], v[156:159], v[124:127]\n\t" \
    "buffer_load_dwordx4 v[136:139], %13, %8, %5 offen offset:2048 sc1\n\t" \
    "v_mfma_f32_16x16x32_bf16 v[120:123], v[204:207], v[152:155], v[120:123]\n\t" \
    "v_mfma_f32_16x16x32_bf16 v[116:119], v[204:207], v[148:151], v[116:119]\n\t" \
    "buffer_load_dwordx4 v[140:143], %13, %8, %5 offen offset:3072 sc1\n\t" \
    "s_add_u32 %3, %3, 1\n\t" \
    "s_cmp_eq_u32 %3, 13\n\t" \
    "s_cselect_b32 %3, 0, %3\n\t" \
    "v_mfma_f32_16x16x32_bf16 v[112:115], v[204:207], v[144:147], v[112:115]\n\t" \
    "ds_read_b64_tr_b16 v[204:205], v243 offset:43008\n\t" \
    "ds_read_b64_tr_b16 v[206:207], v243 offset:45056\n\t" \
    "s_waitcnt vmcnt(12)\n\t" \
    "s_waitcnt lgkmcnt(6)\n\t" \
    "v_mfma_f32_16x16x32_bf16 v[64:67], v[192:195], v[160:163], v[64:67]\n\t" \
    "s_add_u32 m0, %10, 0\n\t" \
    "s_add_u32 %4, %21, 0\n\t" \
    "buffer_load_dwordx4 %20, %19, %4 offen lds\n\t" \
    "v_mfma_f32_16x16x32_bf16 v[68:71], v[192:195], v[164:167], v[68:71]\n\t" \
    "v_mfma_f32_16x16x32_bf16 v[72:75], v[192:195], v[168:171], v[72:75]\n\t" \
    "s_add_u32 m0, %10, 1024\n\t" \
    "s_add_u32 %4, %21, 8192\n\t" \
    "buffer_load_dwordx4 %20, %19, %4 offen lds\n\t" \
    "v_mfma_f32_16x16x32_bf16 v[76:79], v[192:195], v[172:175], v[76:79]\n\t" \
    "ds_read_b64_tr_b16 v[192:193], v240 offset:0\n\t" \
    "ds_read_b64_tr_b16 v[194:195], v240 offset:2048\n\t" \
    "s_waitcnt lgkmcnt(6)\n\t" \
    "v_mfma_f32_16x16x32_bf16 v[92:95], v[196:199], v[172:175], v[92:95]\n\t" \
    "s_add_u32 m0, %10, 2048\n\t" \
    "s_add_u32 %4, %21, 16384\n\t" \
    "buffer_load_dwordx4 %20, %19, %4 offen lds\n\t" \
    "v_mfma_f32_16x16x32_bf16 v[88:91], v[196:199], v[168:171], v[88:91]\n\t" \
    "v_mfma_f32_16x16x32_bf16 v[84:87], v[196:199], v[164:167], v[84:87]\n\t" \
    "s_add_u32 m0, %10, 3072\n\t" \
    "s_add_u32 %4, %21, 24576\n\t" \
    "buffer_load_dwordx4 %20, %19, %4 offen lds\n\t" \
    "v_mfma_f32_16x16x32_bf16 v[80:83], v[196:199], v[160:163], v[80:83]\n\t" \
    "ds_read_b64_tr_b16 v[196:197], v241 offset:0\n\t" \
    "ds_read_b64_tr_b16 v[198:199], v241 offset:2048\n\t" \
    "s_waitcnt lgkmcnt(6)\n\t" \
    "v_mfma_f32_16x16x32_bf16 v[96:99], v[200:203], v[160:163], v[96:99]\n\t" \
    "v_mfma_f32_16x16x32_bf16 v[100:103], v[200:203], v[164:167], v[100:103]\n\t" \
    "v_mfma_f32_16x16x32_bf16 v[104:107], v[200:203], v[168:171], v[104:107]\n\t" \
    "v_mfma_f32_16x16x32_bf16 v[108:111], v[200:203], v[172:175], v[108:111]\n\t" \
    "ds_read_b64_tr_b16 v[200:201], v242 offset:0\n\t" \
    "ds_read_b64_tr_b16 v[202:203], v242 offset:2048\n\t" \
    "s_waitcnt lgkmcnt(6)\n\t" \
    "v_mfma_f32_16x16x32_bf16 v[124:127], v[204:207], v[172:175], v[124:127]\n\t" \
    "v_mfma_f32_16x16x32_bf16 v[120:123], v[204:207], v[168:171], v[120:123]\n\t" \
    "v_mfma_f32_16x16x32_bf16 v[116:119], v[204:207], v[164:167], v[116:119]\n\t" \
    "v_mfma_f32_16x16x32_bf16 v[112:115], v[204:207], v[160:163], v[112:115]\n\t" \
    "ds_read_b64_tr_b16 v[204:205], v243 offset:0\n\t" \
    "ds_read_b64_tr_b16 v[206:207], v243 offset:2048\n\t" \
    "s_waitcnt vmcnt(8)\n\t" \
    "s_waitcnt lgkmcnt(6)\n\t" \
    "v_mfma_f32_16x16x32_bf16 v[64:67], v[192:195], v[176:179], v[64:67]\n\t" \
    "s_add_u32 m0, %10, 4096\n\t" \
    "s_add_u32 %4, %21, 32768\n\t" \
    "buffer_load_dwordx4 %20, %19, %4 offen lds\n\t" \
    "v_mfma_f32_16x16x32_bf16 v[68:71], v[192:195], v[180:183], v[68:71]\n\t" \
    "v_mfma_f32_16x16x32_bf16 v[72:75], v[192:195], v[184:187], v[72:75]\n\t" \
    "s_add_u32 m0, %10, 5120\n\t" \
    "s_add_u32 %4, %21, 40960\n\t" \
    "buffer_load_dwordx4 %20, %19, %4 offen lds\n\t" \
    "v_mfma_f32_16x16x32_bf16 v[76:79], v[192:195], v[188:191], v[76:79]\n\t" \
    "ds_read_b64_tr_b16 v[192:193], v240 offset:4096\n\t" \
    "ds_read_b64_tr_b16 v[194:195], v240 offset:6144\n\t" \
    "s_waitcnt lgkmcnt(6)\n\t" \
    "v_mfma_f32_16x16x32_bf16 v[92:95], v[196:199], v[188:191], v[92:95]\n\t" \
    "s_add_u32 m0, %10, 6144\n\t" \
    "s_add_u32 %4, %21, 49152\n\t" \
    "buffer_load_dwordx4 %20, %19, %4 offen lds\n\t" \
    "v_mfma_f32_16x16x32_bf16 v[88:91], v[196:199], v[184:187], v[88:91]\n\t" \
    "v_mfma_f32_16x16x32_bf16 v[84:87], v[196:199], v[180:183], v[84:87]\n\t" \
    "s_add_u32 m0, %10, 7168\n\t" \
    "s_add_u32 %4, %21, 57344\n\t" \
    "buffer_load_dwordx4 %20, %19, %4 offen lds\n\t" \
    "v_mfma_f32_16x16x32_bf16 v[80:83], v[196:199], v[176:179], v[80:83]\n\t" \
    "ds_read_b64_tr_b16 v[196:197], v241 offset:4096\n\t" \
    "ds_read_b64_tr_b16 v[198:199], v241 offset:6144\n\t" \
    "s_waitcnt lgkmcnt(6)\n\t" \
    "v_mfma_f32_16x16x32_bf16 v[96:99], v[200:203], v[176:179], v[96:99]\n\t" \
    "v_mfma_f32_16x16x32_bf16 v[100:103], v[200:203], v[180:183], v[100:103]\n\t" \
    "v_mfma_f32_16x16x32_bf16 v[104:107], v[200:203], v[184:187], v[104:107]\n\t" \
    "v_mfma_f32_16x16x32_bf16 v[108:111], v[200:203], v[188:191], v[108:111]\n\t" \
    "ds_read_b64_tr_b16 v[200:201], v242 offset:4096\n\t" \
    "ds_read_b64_tr_b16 v[202:203], v242 offset:6144\n\t" \
    "s_waitcnt lgkmcnt(6)\n\t" \
    "v_mfma_f32_16x16x32_bf16 v[124:127], v[204:207], v[188:191], v[124:127]\n\t" \
    "v_mfma_f32_16x16x32_bf16 v[120:123], v[204:207], v[184:187], v[120:123]\n\t" \
    "v_mfma_f32_16x16x32_bf16 v[116:119], v[204:207], v[180:183], v[116:119]\n\t" \
    "v_mfma_f32_16x16x32_bf16 v[112:115], v[204:207], v[176:179], v[112:115]\n\t" \
    "ds_read_b64_tr_b16 v[204:205], v243 offset:4096\n\t" \
    "ds_read_b64_tr_b16 v[206:207], v243 offset:6144\n\t" \
    "s_waitcnt vmcnt(8)\n\t" \
    "s_waitcnt lgkmcnt(6)\n\t" \
    "v_mfma_f32_16x16x32_bf16 v[64:67], v[192:195], v[128:131], v[64:67]\n\t" \
    "v_mfma_f32_16x16x32_bf16 v[68:71], v[192:195], v[132:135], v[68:71]\n\t" \
    "v_mfma_f32_16x16x32_bf16 v[72:75], v[192:195], v[136:139], v[72:75]\n\t" \
    "v_mfma_f32_16x16x32_bf16 v[76:79], v[192:195], v[140:143], v[76:79]\n\t" \
    "s_waitcnt lgkmcnt(4)\n\t" \
    "v_mfma_f32_16x16x32_bf16 v[92:95], v[196:199], v[140:143], v[92:95]\n\t" \
    "v_mfma_f32_16x16x32_bf16 v[88:91], v[196:199], v[136:139], v[88:91]\n\t" \
    "v_mfma_f32_16x16x32_bf16 v[84:87], v[196:199], v[132:135], v[84:87]\n\t" \
    "v_mfma_f32_16x16x32_bf16 v[80:83], v[196:199], v[128:131], v[80:83]\n\t" \
    "s_waitcnt lgkmcnt(2)\n\t" \
    "v_mfma_f32_16x16x32_bf16 v[96:99], v[200:203], v[128:131], v[96:99]\n\t" \
    "v_mfma_f32_16x16x32_bf16 v[100:103], v[200:203], v[132:135], v[100:103]\n\t" \
    "v_mfma_f32_16x16x32_bf16 v[104:107], v[200:203], v[136:139], v[104:107]\n\t" \
    "v_mfma_f32_16x16x32_bf16 v[108:111], v[200:203], v[140:143], v[108:111]\n\t" \
    "s_waitcnt lgkmcnt(0)\n\t" \
    "v_mfma_f32_16x16x32_bf16 v[124:127], v[204:207], v[140:143], v[124:127]\n\t" \
    "v_mfma_f32_16x16x32_bf16 v[120:123], v[204:207], v[136:139], v[120:123]\n\t" \
    "v_mfma_f32_16x16x32_bf16 v[116:119], v[204:207], v[132:135], v[116:119]\n\t" \
    "v_mfma_f32_16x16x32_bf16 v[112:115], v[204:207], v[128:131], v[112:115]\n\t" \
    "s_setprio 2\n\t" \
    "s_waitcnt vmcnt(0)\n\t" \
    "ds_read_b64_tr_b16 v[204:205], v244 offset:0\n\t" \
    "ds_read_b64_tr_b16 v[206:207], v244 offset:1024\n\t" \
    "ds_read_b64_tr_b16 v[208:209], v245 offset:0\n\t" \
    "ds_read_b64_tr_b16 v[210:211], v245 offset:1024\n\t" \
    "ds_read_b64_tr_b16 v[212:213], v246 offset:0\n\t" \
    "ds_read_b64_tr_b16 v[214:215], v246 offset:1024\n\t" \
    "ds_read_b64_tr_b16 v[216:217], v247 offset:0\n\t" \
    "ds_read_b64_tr_b16 v[218:219], v247 offset:1024\n\t" \
    "ds_read_b64_tr_b16 v[220:221], v244 offset:4096\n\t" \
    "ds_read_b64_tr_b16 v[222:223], v244 offset:5120\n\t" \
    "ds_read_b64_tr_b16 v[224:225], v245 offset:4096\n\t" \
    "ds_read_b64_tr_b16 v[226:227], v245 offset:5120\n\t" \
    "ds_read_b64_tr_b16 v[228:229], v246 offset:4096\n\t" \
    "ds_read_b64_tr_b16 v[230:231], v246 offset:5120\n\t" \
    "ds_read_b64_tr_b16 v[232:233], v247 offset:4096\n\t" \
    "ds_read_b64_tr_b16 v[234:235], v247 offset:5120\n\t" \
    "s_waitcnt lgkmcnt(0)\n\t" \
    "s_nop 7\n\t" \
    "92:\n\t"
#define CLOB_DOWN "v128","v129","v130","v131","v132","v133","v134","v135","v136","v137","v138","v139","v140","v141","v142","v143","v144","v145","v146","v147","v148","v149","v150","v151","v152","v153","v154","v155","v156","v157","v158","v159","v160","v161","v162","v163","v164","v165","v166","v167","v168","v169","v170","v171","v172","v173","v174","v175","v176","v177","v178","v179","v180","v181","v182","v183","v184","v185","v186","v187","v188","v189","v190","v191","v192","v193","v194","v195","v196","v197","v198","v199","v200","v201","v202","v203","v236","v237","v238","v239","v240","v241","v242","v243","v244","v245","v246","v247","v249","v250","v251"
#define ASM_ATTN \
    "s_add_u32 %0, %8, 0\n\t" \
    "s_and_b32 %0, %0, 31\n\t" \
    "s_lshl_b32 %0, %0, 5\n\t" \
    "v_add_u32 v120, %0, v198\n\t" \
    "s_add_u32 %0, %8, 4\n\t" \
    "s_and_b32 %0, %0, 31\n\t" \
    "s_lshl_b32 %0, %0, 5\n\t" \
    "v_add_u32 v121, %0, v198\n\t" \
    "s_add_u32 %0, %8, 8\n\t" \
    "s_and_b32 %0, %0, 31\n\t" \
    "s_lshl_b32 %0, %0, 5\n\t" \
    "v_add_u32 v122, %0, v198\n\t" \
    "s_add_u32 %0, %8, 12\n\t" \
    "s_and_b32 %0, %0, 31\n\t" \
    "s_lshl_b32 %0, %0, 5\n\t" \
    "v_add_u32 v123, %0, v198\n\t" \
    "s_add_u32 %0, %8, 16\n\t" \
    "s_and_b32 %0, %0, 31\n\t" \
    "s_lshl_b32 %0, %0, 5\n\t" \
    "v_add_u32 v124, %0, v198\n\t" \
    "s_add_u32 %0, %8, 20\n\t" \
    "s_and_b32 %0, %0, 31\n\t" \
    "s_lshl_b32 %0, %0, 5\n\t" \
    "v_add_u32 v125, %0, v198\n\t" \
    "s_add_u32 %0, %8, 24\n\t" \
    "s_and_b32 %0, %0, 31\n\t" \
    "s_lshl_b32 %0, %0, 5\n\t" \
    "v_add_u32 v126, %0, v198\n\t" \
    "s_add_u32 %0, %8, 28\n\t" \
    "s_and_b32 %0, %0, 31\n\t" \
    "s_lshl_b32 %0, %0, 5\n\t" \
    "v_add_u32 v127, %0, v198\n\t" \
    "v_mov_b32 v72, v2\n\t" \
    "v_mov_b32 v73, v2\n\t" \
    "v_mov_b32 v74, v2\n\t" \
    "v_mov_b32 v75, v2\n\t" \
    "v_mov_b32 v80, v2\n\t" \
    "v_mov_b32 v81, v2\n\t" \
    "v_mov_b32 v82, v2\n\t" \
    "v_mov_b32 v83, v2\n\t" \
    "v_mov_b32 v88, v2\n\t" \
    "v_mov_b32 v89, v2\n\t" \
    "v_mov_b32 v90, v2\n\t" \
    "v_mov_b32 v91, v2\n\t" \
    "v_mov_b32 v96, v2\n\t" \
    "v_mov_b32 v97, v2\n\t" \
    "v_mov_b32 v98, v2\n\t" \
    "v_mov_b32 v99, v2\n\t" \
    "v_mov_b32 v104, v2\n\t" \
    "v_mov_b32 v105, v2\n\t" \
    "v_mov_b32 v106, v2\n\t" \
    "v_mov_b32 v107, v2\n\t" \
    "v_mov_b32 v112, v2\n\t" \
    "v_mov_b32 v113, v2\n\t" \
    "v_mov_b32 v114, v2\n\t" \
    "v_mov_b32 v115, v2\n\t" \
    "v_mov_b32 v168, v2\n\t" \
    "v_mov_b32 v169, v2\n\t" \
    "v_mov_b32 v170, v2\n\t" \
    "v_mov_b32 v171, v2\n\t" \
    "v_mov_b32 v176, v2\n\t" \
    "v_mov_b32 v177, v2\n\t" \
    "v_mov_b32 v178, v2\n\t" \
    "v_mov_b32 v179, v2\n\t" \
    "v_mov_b32 v184, v2\n\t" \
    "v_mov_b32 v185, v2\n\t" \
    "v_mov_b32 v186, v2\n\t" \
    "v_mov_b32 v187, v2\n\t" \
    "v_mov_b32 v192, v2\n\t" \
    "v_mov_b32 v193, v2\n\t" \
    "v_mov_b32 v194, v2\n\t" \
    "v_mov_b32 v195, v2\n\t" \
    "v_mov_b32 v248, 0\n\t" \
    "v_mov_b32 v249, 0\n\t" \
    "v_mov_b32 v250, 0\n\t" \
    "v_mov_b32 v251, 0\n\t" \
    "v_mov_b32 v252, 0xdddd\n\t" \
    "v_mov_b32 v0, 0\n\t" \
    "s_mov_b32 %2, 0\n\t" \
    "4:\n\t" \
    "v_mov_b32 v216, %7\n\t" \
    "ds_read_b32 v216, v216\n\t" \
    "s_waitcnt lgkmcnt(0)\n\t" \
    "v_readfirstlane_b32 vcc_lo, v216\n\t" \
    "s_add_u32 %2, %2, 1\n\t" \
    "s_cmp_ge_u32 vcc_lo, 7\n\t" \
    "s_cbranch_scc1 40f\n\t" \
    "s_sleep 1\n\t" \
    "s_cmp_lt_u32 %2, 0x400000\n\t" \
    "s_cbranch_scc1 4b\n\t" \
    "40:\n\t" \
    "s_add_u32 %0, %7, 8\n\t" \
    "s_mov_b32 %2, 0\n\t" \
    "5:\n\t" \
    "v_mov_b32 v216, %0\n\t" \
    "ds_read_b32 v216, v216\n\t" \
    "s_waitcnt lgkmcnt(0)\n\t" \
    "v_readfirstlane_b32 vcc_lo, v216\n\t" \
    "s_add_u32 %2, %2, 1\n\t" \
    "s_cmp_ge_u32 vcc_lo, 7\n\t" \
    "s_cbranch_scc1 50f\n\t" \
    "s_sleep 1\n\t" \
    "s_cmp_lt_u32 %2, 0x400000\n\t" \
    "s_cbranch_scc1 5b\n\t" \
    "50:\n\t" \
    "s_setprio 3\n\t" \
    "ds_read_b64_tr_b16 v[128:129], v196 offset:0\n\t" \
    "ds_read_b64_tr_b16 v[130:131], v196 offset:512\n\t" \
    "ds_read_b128 v[132:135], v120 offset:0\n\t" \
    "ds_read_b64_tr_b16 v[136:137], v196 offset:2048\n\t" \
    "ds_read_b64_tr_b16 v[138:139], v196 offset:2560\n\t" \
    "ds_read_b128 v[140:143], v120 offset:32\n\t" \
    "ds_read_b64_tr_b16 v[144:145], v196 offset:4096\n\t" \
    "ds_read_b64_tr_b16 v[146:147], v196 offset:4608\n\t" \
    "ds_read_b128 v[148:151], v120 offset:64\n\t" \
    "ds_read_b64_tr_b16 v[152:153], v196 offset:6144\n\t" \
    "ds_read_b64_tr_b16 v[154:155], v196 offset:6656\n\t" \
    "ds_read_b128 v[156:159], v120 offset:96\n\t" \
    "ds_read_b64_tr_b16 v[160:161], v196 offset:8192\n\t" \
    "ds_read_b64_tr_b16 v[162:163], v196 offset:8704\n\t" \
    "ds_read_b128 v[164:167], v121 offset:0\n\t" \
    "s_waitcnt lgkmcnt(12)\n\t" \
    "v_mfma_f32_32x32x16_bf16 v[228:243], v[128:131], v[132:135], 0\n\t" \
    "ds_read_b64_tr_b16 v[128:129], v196 offset:10240\n\t" \
    "ds_read_b64_tr_b16 v[130:131], v196 offset:10752\n\t" \
    "ds_read_b128 v[132:135], v121 offset:32\n\t" \
    "s_waitcnt lgkmcnt(12)\n\t" \
    "v_mfma_f32_32x32x16_bf16 v[228:243], v[136:139], v[140:143], v[228:243]\n\t" \
    "ds_read_b64_tr_b16 v[136:137], v196 offset:12288\n\t" \
    "ds_read_b64_tr_b16 v[138:139], v196 offset:12800\n\t" \
    "ds_read_b128 v[140:143], v121 offset:64\n\t" \
    "s_waitcnt lgkmcnt(12)\n\t" \
    "v_mfma_f32_32x32x16_bf16 v[228:243], v[144:147], v[148:151], v[228:243]\n\t" \
    "ds_read_b64_tr_b16 v[144:145], v196 offset:14336\n\t" \
    "ds_read_b64_tr_b16 v[146:147], v196 offset:14848\n\t" \
    "ds_read_b128 v[148:151], v121 offset:96\n\t" \
    "s_waitcnt lgkmcnt(12)\n\t" \
    "v_mfma_f32_32x32x16_bf16 v[228:243], v[152:155], v[156:159], v[228:243]\n\t" \
    "ds_read_b64_tr_b16 v[152:153], v196 offset:16384\n\t" \
    "ds_read_b64_tr_b16 v[154:155], v196 offset:16896\n\t" \
    "ds_read_b128 v[156:159], v122 offset:0\n\t" \
    "s_waitcnt lgkmcnt(12)\n\t" \
    "v_mfma_f32_32x32x16_bf16 v[228:243], v[160:163], v[164:167], v[228:243]\n\t" \
    "s_add_u32 %0, %7, 20\n\t" \
    "v_mov_b32 v216, %0\n\t" \
    "v_mov_b32 v217, 1\n\t" \
    "s_mov_b64 exec, 1\n\t" \
    "ds_add_u32 v216, v217\n\t" \
    "s_mov_b64 exec, -1\n\t" \
    "ds_read_b64_tr_b16 v[160:161], v196 offset:18432\n\t" \
    "ds_read_b64_tr_b16 v[162:163], v196 offset:18944\n\t" \
    "ds_read_b128 v[164:167], v122 offset:32\n\t" \
    "s_waitcnt lgkmcnt(12)\n\t" \
    "v_mfma_f32_32x32x16_bf16 v[228:243], v[128:131], v[132:135], v[228:243]\n\t" \
    "ds_read_b64_tr_b16 v[128:129], v196 offset:20480\n\t" \
    "ds_read_b64_tr_b16 v[130:131], v196 offset:20992\n\t" \
    "ds_read_b128 v[132:135], v122 offset:64\n\t" \
    "s_waitcnt lgkmcnt(12)\n\t" \
    "v_mfma_f32_32x32x16_bf16 v[228:243], v[136:139], v[140:143], v[228:243]\n\t" \
    "ds_read_b64_tr_b16 v[136:137], v196 offset:22528\n\t" \
    "ds_read_b64_tr_b16 v[138:139], v196 offset:23040\n\t" \
    "ds_read_b128 v[140:143], v122 offset:96\n\t" \
    "s_waitcnt lgkmcnt(12)\n\t" \
    "v_mfma_f32_32x32x16_bf16 v[228:243], v[144:147], v[148:151], v[228:243]\n\t" \
    "ds_read_b64_tr_b16 v[144:145], v196 offset:24576\n\t" \
    "ds_read_b64_tr_b16 v[146:147], v196 offset:25088\n\t" \
    "ds_read_b128 v[148:151], v123 offset:0\n\t" \
    "s_waitcnt lgkmcnt(12)\n\t" \
    "v_mfma_f32_32x32x16_bf16 v[228:243], v[152:155], v[156:159], v[228:243]\n\t" \
    "s_cmp_eq_u32 %5, 65536\n\t" \
    "s_cbranch_scc1 16f\n\t" \
    "s_waitcnt vmcnt(0)\n\t" \
    "s_add_u32 %0, %7, 12\n\t" \
    "v_mov_b32 v216, %0\n\t" \
    "v_mov_b32 v217, 1\n\t" \
    "s_mov_b64 exec, 1\n\t" \
    "ds_add_u32 v216, v217\n\t" \
    "s_mov_b64 exec, -1\n\t" \
    "16:\n\t" \
    "buffer_load_dwordx2 v[202:203], v201, %4, 0 offen sc1\n\t" \
    "v_mov_b32 v216, %7\n\t" \
    "ds_read_b32 v222, v216 offset:4\n\t" \
    "ds_read_b64 v[220:221], v216 offset:256\n\t" \
    "ds_read_b64_tr_b16 v[152:153], v196 offset:26624\n\t" \
    "ds_read_b64_tr_b16 v[154:155], v196 offset:27136\n\t" \
    "ds_read_b128 v[156:159], v123 offset:32\n\t" \
    "s_waitcnt lgkmcnt(12)\n\t" \
    "v_mfma_f32_32x32x16_bf16 v[228:243], v[160:163], v[164:167], v[228:243]\n\t" \
    "s_add_u32 %0, %7, 12\n\t" \
    "s_mov_b32 %2, 0\n\t" \
    "6:\n\t" \
    "v_mov_b32 v216, %0\n\t" \
    "ds_read_b32 v216, v216\n\t" \
    "s_waitcnt lgkmcnt(0)\n\t" \
    "v_readfirstlane_b32 vcc_lo, v216\n\t" \
    "s_add_u32 %2, %2, 1\n\t" \
    "s_cmp_ge_u32 vcc_lo, 3\n\t" \
    "s_cbranch_scc1 60f\n\t" \
    "s_sleep 1\n\t" \
    "s_cmp_lt_u32 %2, 0x400000\n\t" \
    "s_cbranch_scc1 6b\n\t" \
    "60:\n\t" \
    "ds_read_b64_tr_b16 v[160:161], v196 offset:28672\n\t" \
    "ds_read_b64_tr_b16 v[162:163], v196 offset:29184\n\t" \
    "ds_read_b128 v[164:167], v123 offset:64\n\t" \
    "s_waitcnt lgkmcnt(12)\n\t" \
    "v_mfma_f32_32x32x16_bf16 v[228:243], v[128:131], v[132:135], v[228:243]\n\t" \
    "ds_read_b64_tr_b16 v[128:129], v196 offset:30720\n\t" \
    "ds_read_b64_tr_b16 v[130:131], v196 offset:31232\n\t" \
    "ds_read_b128 v[132:135], v123 offset:96\n\t" \
    "s_waitcnt lgkmcnt(12)\n\t" \
    "v_mfma_f32_32x32x16_bf16 v[228:243], v[136:139], v[140:143], v[228:243]\n\t" \
    "ds_read_b64_tr_b16 v[136:137], v196 offset:32768\n\t" \
    "ds_read_b64_tr_b16 v[138:139], v196 offset:33280\n\t" \
    "ds_read_b128 v[140:143], v124 offset:0\n\t" \
    "s_waitcnt lgkmcnt(12)\n\t" \
    "v_mfma_f32_32x32x16_bf16 v[228:243], v[144:147], v[148:151], v[228:243]\n\t" \
    "ds_read_b64_tr_b16 v[144:145], v196 offset:34816\n\t" \
    "ds_read_b64_tr_b16 v[146:147], v196 offset:35328\n\t" \
    "ds_read_b128 v[148:151], v124 offset:32\n\t" \
    "s_waitcnt lgkmcnt(12)\n\t" \
    "v_mfma_f32_32x32x16_bf16 v[228:243], v[152:155], v[156:159], v[228:243]\n\t" \
    "ds_read_b64_tr_b16 v[152:153], v196 offset:36864\n\t" \
    "ds_read_b64_tr_b16 v[154:155], v196 offset:37376\n\t" \
    "ds_read_b128 v[156:159], v124 offset:64\n\t" \
    "s_waitcnt lgkmcnt(12)\n\t" \
    "v_mfma_f32_32x32x16_bf16 v[228:243], v[160:163], v[164:167], v[228:243]\n\t" \
    "ds_read_b64_tr_b16 v[160:161], v196 offset:38912\n\t" \
    "ds_read_b64_tr_b16 v[162:163], v196 offset:39424\n\t" \
    "ds_read_b128 v[164:167], v124 offset:96\n\t" \
    "s_waitcnt lgkmcnt(12)\n\t" \
    "v_mfma_f32_32x32x16_bf16 v[228:243], v[128:131], v[132:135], v[228:243]\n\t" \
    "ds_read_b64_tr_b16 v[128:129], v196 offset:40960\n\t" \
    "ds_read_b64_tr_b16 v[130:131], v196 offset:41472\n\t" \
    "ds_read_b128 v[132:135], v125 offset:0\n\t" \
    "s_waitcnt lgkmcnt(12)\n\t" \
    "v_mfma_f32_32x32x16_bf16 v[228:243], v[136:139], v[140:143], v[228:243]\n\t" \
    "ds_read_b64_tr_b16 v[136:137], v196 offset:43008\n\t" \
    "ds_read_b64_tr_b16 v[138:139], v196 offset:43520\n\t" \
    "ds_read_b128 v[140:143], v125 offset:32\n\t" \
    "s_waitcnt lgkmcnt(12)\n\t" \
    "v_mfma_f32_32x32x16_bf16 v[228:243], v[144:147], v[148:151], v[228:243]\n\t" \
    "ds_read_b64_tr_b16 v[144:145], v196 offset:45056\n\t" \
    "ds_read_b64_tr_b16 v[146:147], v196 offset:45568\n\t" \
    "ds_read_b128 v[148:151], v125 offset:64\n\t" \
    "s_waitcnt lgkmcnt(12)\n\t" \
    "v_mfma_f32_32x32x16_bf16 v[228:243], v[152:155], v[156:159], v[228:243]\n\t" \
    "ds_read_b64_tr_b16 v[152:153], v196 offset:47104\n\t" \
    "ds_read_b64_tr_b16 v[154:155], v196 offset:47616\n\t" \
    "ds_read_b128 v[156:159], v125 offset:96\n\t" \
    "s_waitcnt lgkmcnt(12)\n\t" \
    "v_mfma_f32_32x32x16_bf16 v[228:243], v[160:163], v[164:167], v[228:243]\n\t" \
    "ds_read_b64_tr_b16 v[160:161], v196 offset:49152\n\t" \
    "ds_read_b64_tr_b16 v[162:163], v196 offset:49664\n\t" \
    "ds_read_b128 v[164:167], v126 offset:0\n\t" \
    "s_waitcnt lgkmcnt(12)\n\t" \
    "v_mfma_f32_32x32x16_bf16 v[228:243], v[128:131], v[132:135], v[228:243]\n\t" \
    "ds_read_b64_tr_b16 v[128:129], v196 offset:51200\n\t" \
    "ds_read_b64_tr_b16 v[130:131], v196 offset:51712\n\t" \
    "ds_read_b128 v[132:135], v126 offset:32\n\t" \
    "s_waitcnt lgkmcnt(12)\n\t" \
    "v_mfma_f32_32x32x16_bf16 v[228:243], v[136:139], v[140:143], v[228:243]\n\t" \
    "ds_read_b64_tr_b16 v[136:137], v196 offset:53248\n\t" \
    "ds_read_b64_tr_b16 v[138:139], v196 offset:53760\n\t" \
    "ds_read_b128 v[140:143], v126 offset:64\n\t" \
    "s_waitcnt lgkmcnt(12)\n\t" \
    "v_mfma_f32_32x32x16_bf16 v[228:243], v[144:147], v[148:151], v[228:243]\n\t" \
    "s_waitcnt vmcnt(0)\n\t" \
    "v_readfirstlane_b32 s64, v220\n\t" \
    "v_readfirstlane_b32 s65, v221\n\t" \
    "v_readfirstlane_b32 s63, v222\n\t" \
    "s_nop 0\n\t" \
    "v_cmp_eq_u64 vcc, s[64:65], v[202:203]\n\t" \
    "s_cmp_eq_u32 s63, 0\n\t" \
    "s_cselect_b32 vcc_lo, 0, vcc_lo\n\t" \
    "s_mov_b32 s63, 0\n\t" \
    "s_cmp_eq_u64 vcc, -1\n\t" \
    "s_cbranch_scc0 12f\n\t" \
    "s_add_u32 %2, %9, 0\n\t" \
    "s_and_b32 %2, %2, 15\n\t" \
    "s_lshl_b32 %2, %2, 10\n\t" \
    "s_add_u32 %0, %2, %5\n\t" \
    "s_add_u32 %1, %2, %6\n\t" \
    "buffer_load_dwordx4 v[4:7], v1, %4, %0 offen sc1\n\t" \
    "buffer_load_dwordx4 v[68:71], v1, %4, %1 offen sc1\n\t" \
    "s_add_u32 %2, %9, 1\n\t" \
    "s_and_b32 %2, %2, 15\n\t" \
    "s_lshl_b32 %2, %2, 10\n\t" \
    "s_add_u32 %0, %2, %5\n\t" \
    "s_add_u32 %1, %2, %6\n\t" \
    "buffer_load_dwordx4 v[8:11], v1, %4, %0 offen sc1\n\t" \
    "buffer_load_dwordx4 v[76:79], v1, %4, %1 offen sc1\n\t" \
    "s_add_u32 %2, %9, 2\n\t" \
    "s_and_b32 %2, %2, 15\n\t" \
    "s_lshl_b32 %2, %2, 10\n\t" \
    "s_add_u32 %0, %2, %5\n\t" \
    "s_add_u32 %1, %2, %6\n\t" \
    "buffer_load_dwordx4 v[12:15], v1, %4, %0 offen sc1\n\t" \
    "buffer_load_dwordx4 v[84:87], v1, %4, %1 offen sc1\n\t" \
    "s_mov_b32 s63, 1\n\t" \
    "12:\n\t" \
    "ds_read_b64_tr_b16 v[144:145], v196 offset:55296\n\t" \
    "ds_read_b64_tr_b16 v[146:147], v196 offset:55808\n\t" \
    "ds_read_b128 v[148:151], v126 offset:96\n\t" \
    "s_waitcnt lgkmcnt(12)\n\t" \
    "v_mfma_f32_32x32x16_bf16 v[228:243], v[152:155], v[156:159], v[228:243]\n\t" \
    "ds_read_b64_tr_b16 v[152:153], v196 offset:57344\n\t" \
    "ds_read_b64_tr_b16 v[154:155], v196 offset:57856\n\t" \
    "ds_read_b128 v[156:159], v127 offset:0\n\t" \
    "s_waitcnt lgkmcnt(12)\n\t" \
    "v_mfma_f32_32x32x16_bf16 v[228:243], v[160:163], v[164:167], v[228:243]\n\t" \
    "ds_read_b64_tr_b16 v[160:161], v196 offset:59392\n\t" \
    "ds_read_b64_tr_b16 v[162:163], v196 offset:59904\n\t" \
    "ds_read_b128 v[164:167], v127 offset:32\n\t" \
    "s_waitcnt lgkmcnt(12)\n\t" \
    "v_mfma_f32_32x32x16_bf16 v[228:243], v[128:131], v[132:135], v[228:243]\n\t" \
    "ds_read_b64_tr_b16 v[128:129], v196 offset:61440\n\t" \
    "ds_read_b64_tr_b16 v[130:131], v196 offset:61952\n\t" \
    "ds_read_b128 v[132:135], v127 offset:64\n\t" \
    "s_waitcnt lgkmcnt(12)\n\t" \
    "v_mfma_f32_32x32x16_bf16 v[228:243], v[136:139], v[140:143], v[228:243]\n\t" \
    "ds_read_b64_tr_b16 v[136:137], v197 offset:0\n\t" \
    "ds_read_b64_tr_b16 v[138:139], v197 offset:512\n\t" \
    "ds_read_b128 v[140:143], v127 offset:96\n\t" \
    "s_waitcnt lgkmcnt(12)\n\t" \
    "v_mfma_f32_32x32x16_bf16 v[228:243], v[144:147], v[148:151], v[228:243]\n\t" \
    "s_waitcnt lgkmcnt(9)\n\t" \
    "v_mfma_f32_32x32x16_bf16 v[228:243], v[152:155], v[156:159], v[228:243]\n\t" \
    "s_waitcnt lgkmcnt(6)\n\t" \
    "v_mfma_f32_32x32x16_bf16 v[228:243], v[160:163], v[164:167], v[228:243]\n\t" \
    "s_waitcnt lgkmcnt(3)\n\t" \
    "v_mfma_f32_32x32x16_bf16 v[228:243], v[128:131], v[132:135], v[228:243]\n\t" \
    "s_waitcnt lgkmcnt(0)\n\t" \
    "v_mfma_f32_32x32x16_bf16 v[228:243], v[136:139], v[140:143], v[228:243]\n\t" \
    "s_cmp_eq_u32 s63, 1\n\t" \
    "s_cbranch_scc1 13f\n\t" \
    "buffer_load_dwordx2 v[202:203], v201, %4, 0 offen sc1\n\t" \
    "13:\n\t" \
    "s_add_u32 %0, %7, 24\n\t" \
    "v_mov_b32 v216, %0\n\t" \
    "v_mov_b32 v217, 1\n\t" \
    "s_mov_b64 exec, 1\n\t" \
    "ds_add_u32 v216, v217\n\t" \
    "s_mov_b64 exec, -1\n\t" \
    "s_add_u32 %0, %7, 256\n\t" \
    "v_mov_b32 v204, %0\n\t" \
    "v_mov_b32 v216, %7\n\t" \
    "s_mov_b32 %2, 0\n\t" \
    "7:\n\t" \
    "ds_read_b64 v[218:219], v216 offset:16\n\t" \
    "ds_read_b32 v206, v199\n\t" \
    "ds_read_b64 v[220:221], v204\n\t" \
    "s_waitcnt lgkmcnt(0)\n\t" \
    "v_readfirstlane_b32 s64, v218\n\t" \
    "v_readfirstlane_b32 s65, v219\n\t" \
    "s_add_u32 %2, %2, 1\n\t" \
    "s_lshl_b32 s65, s65, 1\n\t" \
    "s_min_u32 s64, s64, s65\n\t" \
    "s_cmp_ge_u32 s64, 7\n\t" \
    "s_cbranch_scc1 70f\n\t" \
    "s_cmp_lt_u32 %2, 0x400000\n\t" \
    "s_cbranch_scc1 7b\n\t" \
    "70:\n\t" \
    "v_mul_f32 v206, 0x3eb8aa3b, v206\n\t" \
    "v_readfirstlane_b32 s64, v220\n\t" \
    "v_readfirstlane_b32 s65, v221\n\t" \
    "v_mul_f32 v228, v206, v228\n\t" \
    "v_mul_f32 v229, v206, v229\n\t" \
    "v_mul_f32 v230, v206, v230\n\t" \
    "v_mul_f32 v231, v206, v231\n\t" \
    "v_mul_f32 v232, v206, v232\n\t" \
    "v_mul_f32 v233, v206, v233\n\t" \
    "v_mul_f32 v234, v206, v234\n\t" \
    "v_mul_f32 v235, v206, v235\n\t" \
    "v_cvt_pk_bf16_f32 v208, v228, v229\n\t" \
    "v_cvt_pk_bf16_f32 v209, v230, v231\n\t" \
    "v_cvt_pk_bf16_f32 v210, v232, v233\n\t" \
    "v_cvt_pk_bf16_f32 v211, v234, v235\n\t" \
    "v_mul_f32 v236, v206, v236\n\t" \
    "v_mul_f32 v237, v206, v237\n\t" \
    "ds_write_b128 v200, v[208:211]\n\t" \
    "v_mul_f32 v238, v206, v238\n\t" \
    "v_mul_f32 v239, v206, v239\n\t" \
    "v_mul_f32 v240, v206, v240\n\t" \
    "v_mul_f32 v241, v206, v241\n\t" \
    "v_mul_f32 v242, v206, v242\n\t" \
    "v_mul_f32 v243, v206, v243\n\t" \
    "v_cvt_pk_bf16_f32 v212, v236, v237\n\t" \
    "v_cvt_pk_bf16_f32 v213, v238, v239\n\t" \
    "v_cvt_pk_bf16_f32 v214, v240, v241\n\t" \
    "v_cvt_pk_bf16_f32 v215, v242, v243\n\t" \
    "s_nop 0\n\t" \
    "ds_write_b128 v200, v[212:215] offset:256\n\t" \
    "s_add_u32 %0, %7, 28\n\t" \
    "v_mov_b32 v216, %0\n\t" \
    "v_mov_b32 v217, 1\n\t" \
    "s_mov_b64 exec, 1\n\t" \
    "ds_add_u32 v216, v217\n\t" \
    "s_mov_b64 exec, -1\n\t" \
    "v_mov_b32 v120, v2\n\t" \
    "v_mov_b32 v121, v2\n\t" \
    "v_mov_b32 v122, v2\n\t" \
    "v_mov_b32 v123, v2\n\t" \
    "v_mov_b32 v128, v2\n\t" \
    "v_mov_b32 v129, v2\n\t" \
    "v_mov_b32 v130, v2\n\t" \
    "v_mov_b32 v131, v2\n\t" \
    "v_mov_b32 v136, v2\n\t" \
    "v_mov_b32 v137, v2\n\t" \
    "v_mov_b32 v138, v2\n\t" \
    "v_mov_b32 v139, v2\n\t" \
    "v_mov_b32 v144, v2\n\t" \
    "v_mov_b32 v145, v2\n\t" \
    "v_mov_b32 v146, v2\n\t" \
    "v_mov_b32 v147, v2\n\t" \
    "v_mov_b32 v152, v2\n\t" \
    "v_mov_b32 v153, v2\n\t" \
    "v_mov_b32 v154, v2\n\t" \
    "v_mov_b32 v155, v2\n\t" \
    "v_mov_b32 v160, v2\n\t" \
    "v_mov_b32 v161, v2\n\t" \
    "v_mov_b32 v162, v2\n\t" \
    "v_mov_b32 v163, v2\n\t" \
    "s_cmp_eq_u32 s63, 1\n\t" \
    "s_cbranch_scc1 14f\n\t" \
    "s_mov_b32 %2, 0\n\t" \
    "8:\n\t" \
    "s_waitcnt vmcnt(0)\n\t" \
    "v_cmp_eq_u64 vcc, s[64:65], v[202:203]\n\t" \
    "s_add_u32 %2, %2, 1\n\t" \
    "s_cmp_eq_u64 vcc, -1\n\t" \
    "s_cbranch_scc1 80f\n\t" \
    "s_sleep 4\n\t" \
    "buffer_load_dwordx2 v[202:203], v201, %4, 0 offen sc1\n\t" \
    "s_cmp_lt_u32 %2, 0x400000\n\t" \
    "s_cbranch_scc1 8b\n\t" \
    "s_waitcnt vmcnt(0)\n\t" \
    "80:\n\t" \
    "s_add_u32 %2, %9, 0\n\t" \
    "s_and_b32 %2, %2, 15\n\t" \
    "s_lshl_b32 %2, %2, 10\n\t" \
    "s_add_u32 %0, %2, %5\n\t" \
    "s_add_u32 %1, %2, %6\n\t" \
    "buffer_load_dwordx4 v[4:7], v1, %4, %0 offen sc1\n\t" \
    "buffer_load_dwordx4 v[68:71], v1, %4, %1 offen sc1\n\t" \
    "s_add_u32 %2, %9, 1\n\t" \
    "s_and_b32 %2, %2, 15\n\t" \
    "s_lshl_b32 %2, %2, 10\n\t" \
    "s_add_u32 %0, %2, %5\n\t" \
    "s_add_u32 %1, %2, %6\n\t" \
    "buffer_load_dwordx4 v[8:11], v1, %4, %0 offen sc1\n\t" \
    "buffer_load_dwordx4 v[76:79], v1, %4, %1 offen sc1\n\t" \
    "s_add_u32 %2, %9, 2\n\t" \
    "s_and_b32 %2, %2, 15\n\t" \
    "s_lshl_b32 %2, %2, 10\n\t" \
    "s_add_u32 %0, %2, %5\n\t" \
    "s_add_u32 %1, %2, %6\n\t" \
    "buffer_load_dwordx4 v[12:15], v1, %4, %0 offen sc1\n\t" \
    "buffer_load_dwordx4 v[84:87], v1, %4, %1 offen sc1\n\t" \
    "14:\n\t" \
    "s_add_u32 %0, %7, 28\n\t" \
    "s_mov_b32 %2, 0\n\t" \
    "9:\n\t" \
    "v_mov_b32 v216, %0\n\t" \
    "ds_read_b32 v216, v216\n\t" \
    "s_waitcnt lgkmcnt(0)\n\t" \
    "v_readfirstlane_b32 vcc_lo, v216\n\t" \
    "s_add_u32 %2, %2, 1\n\t" \
    "s_cmp_ge_u32 vcc_lo, 4\n\t" \
    "s_cbranch_scc1 90f\n\t" \
    "s_cmp_lt_u32 %2, 0x400000\n\t" \
    "s_cbranch_scc1 9b\n\t" \
    "90:\n\t" \
    "s_setprio 1\n\t" \
    "ds_read_b128 v[244:247], v253\n\t" \
    "v_mfma_f32_32x32x16_bf16 v[228:243], v[248:251], v[248:251], 0\n\t" \
    "s_waitcnt vmcnt(5) lgkmcnt(0)\n\t" \
    "v_mfma_f32_32x32x16_bf16 v[196:211], v[4:7], v[244:247], 0\n\t" \
    "s_nop 11\n\t" \
    "v_exp_f32 v196, v196\n\t" \
    "v_exp_f32 v197, v197\n\t" \
    "v_exp_f32 v198, v198\n\t" \
    "v_exp_f32 v199, v199\n\t" \
    "v_exp_f32 v200, v200\n\t" \
    "s_waitcnt vmcnt(3)\n\t" \
    "v_mfma_f32_32x32x16_bf16 v[212:227], v[8:11], v[244:247], 0\n\t" \
    "v_permlane16_swap_b32 v68, v72\n\t" \
    "v_permlane16_swap_b32 v69, v73\n\t" \
    "v_permlane16_swap_b32 v70, v74\n\t" \
    "v_permlane16_swap_b32 v71, v75\n\t" \
    "v_exp_f32 v201, v201\n\t" \
    "v_exp_f32 v202, v202\n\t" \
    "s_add_u32 %2, %9, 3\n\t" \
    "s_and_b32 %2, %2, 15\n\t" \
    "s_lshl_b32 %2, %2, 10\n\t" \
    "s_add_u32 %0, %2, %5\n\t" \
    "s_add_u32 %1, %2, %6\n\t" \
    "buffer_load_dwordx4 v[16:19], v1, %4, %0 offen sc1\n\t" \
    "v_exp_f32 v203, v203\n\t" \
    "v_exp_f32 v204, v204\n\t" \
    "v_exp_f32 v205, v205\n\t" \
    "v_exp_f32 v206, v206\n\t" \
    "v_smfmac_f32_32x32x32_bf16 v[228:243], v[68:71], v[196:203], v252\n\t" \
    "v_exp_f32 v207, v207\n\t" \
    "v_exp_f32 v208, v208\n\t" \
    "buffer_load_dwordx4 v[92:95], v1, %4, %1 offen sc1\n\t" \
    "v_exp_f32 v209, v209\n\t" \
    "v_exp_f32 v210, v210\n\t" \
    "v_exp_f32 v211, v211\n\t" \
    "v_exp_f32 v212, v212\n\t" \
    "v_exp_f32 v213, v213\n\t" \
    "v_smfmac_f32_32x32x32_bf16 v[228:243], v[72:75], v[204:211], v252\n\t" \
    "v_exp_f32 v214, v214\n\t" \
    "v_exp_f32 v215, v215\n\t" \
    "v_exp_f32 v216, v216\n\t" \
    "s_waitcnt vmcnt(3)\n\t" \
    "v_mfma_f32_32x32x16_bf16 v[196:211], v[12:15], v[244:247], 0\n\t" \
    "v_permlane16_swap_b32 v76, v80\n\t" \
    "v_permlane16_swap_b32 v77, v81\n\t" \
    "v_permlane16_swap_b32 v78, v82\n\t" \
    "v_permlane16_swap_b32 v79, v83\n\t" \
    "v_exp_f32 v217, v217\n\t" \
    "v_exp_f32 v218, v218\n\t" \
    "s_add_u32 %2, %9, 4\n\t" \
    "s_and_b32 %2, %2, 15\n\t" \
    "s_lshl_b32 %2, %2, 10\n\t" \
    "s_add_u32 %0, %2, %5\n\t" \
    "s_add_u32 %1, %2, %6\n\t" \
    "buffer_load_dwordx4 v[20:23], v1, %4, %0 offen sc1\n\t" \
    "v_exp_f32 v219, v219\n\t" \
    "v_exp_f32 v220, v220\n\t" \
    "v_exp_f32 v221, v221\n\t" \
    "v_exp_f32 v222, v222\n\t" \
    "v_smfmac_f32_32x32x32_bf16 v[228:243], v[76:79], v[212:219], v252\n\t" \
    "v_exp_f32 v223, v223\n\t" \
    "v_exp_f32 v224, v224\n\t" \
    "buffer_load_dwordx4 v[100:103], v1, %4, %1 offen sc1\n\t" \
    "v_exp_f32 v225, v225\n\t" \
    "v_exp_f32 v226, v226\n\t" \
    "v_exp_f32 v227, v227\n\t" \
    "v_exp_f32 v196, v196\n\t" \
    "v_exp_f32 v197, v197\n\t" \
    "v_smfmac_f32_32x32x32_bf16 v[228:243], v[80:83], v[220:227], v252\n\t" \
    "v_exp_f32 v198, v198\n\t" \
    "v_exp_f32 v199, v199\n\t" \
    "v_exp_f32 v200, v200\n\t" \
    "s_waitcnt vmcnt(3)\n\t" \
    "v_mfma_f32_32x32x16_bf16 v[212:227], v[16:19], v[244:247], 0\n\t" \
    "v_permlane16_swap_b32 v84, v88\n\t" \
    "v_permlane16_swap_b32 v85, v89\n\t" \
    "v_permlane16_swap_b32 v86, v90\n\t" \
    "v_permlane16_swap_b32 v87, v91\n\t" \
    "v_exp_f32 v201, v201\n\t" \
    "v_exp_f32 v202, v202\n\t" \
    "s_add_u32 %2, %9, 5\n\t" \
    "s_and_b32 %2, %2, 15\n\t" \
    "s_lshl_b32 %2, %2, 10\n\t" \
    "s_add_u32 %0, %2, %5\n\t" \
    "s_add_u32 %1, %2, %6\n\t" \
    "buffer_load_dwordx4 v[24:27], v1, %4, %0 offen sc1\n\t" \
    "v_exp_f32 v203, v203\n\t" \
    "v_exp_f32 v204, v204\n\t" \
    "v_exp_f32 v205, v205\n\t" \
    "v_exp_f32 v206, v206\n\t" \
    "v_smfmac_f32_32x32x32_bf16 v[228:243], v[84:87], v[196:203], v252\n\t" \
    "v_exp_f32 v207, v207\n\t" \
    "v_exp_f32 v208, v208\n\t" \
    "buffer_load_dwordx4 v[108:111], v1, %4, %1 offen sc1\n\t" \
    "v_exp_f32 v209, v209\n\t" \
    "v_exp_f32 v210, v210\n\t" \
    "v_exp_f32 v211, v211\n\t" \
    "v_exp_f32 v212, v212\n\t" \
    "v_exp_f32 v213, v213\n\t" \
    "v_smfmac_f32_32x32x32_bf16 v[228:243], v[88:91], v[204:211], v252\n\t" \
    "v_exp_f32 v214, v214\n\t" \
    "v_exp_f32 v215, v215\n\t" \
    "v_exp_f32 v216, v216\n\t" \
    "s_waitcnt vmcnt(3)\n\t" \
    "v_mfma_f32_32x32x16_bf16 v[196:211], v[20:23], v[244:247], 0\n\t" \
    "v_permlane16_swap_b32 v92, v96\n\t" \
    "v_permlane16_swap_b32 v93, v97\n\t" \
    "v_permlane16_swap_b32 v94, v98\n\t" \
    "v_permlane16_swap_b32 v95, v99\n\t" \
    "v_exp_f32 v217, v217\n\t" \
    "v_exp_f32 v218, v218\n\t" \
    "s_add_u32 %2, %9, 6\n\t" \
    "s_and_b32 %2, %2, 15\n\t" \
    "s_lshl_b32 %2, %2, 10\n\t" \
    "s_add_u32 %0, %2, %5\n\t" \
    "s_add_u32 %1, %2, %6\n\t" \
    "buffer_load_dwordx4 v[28:31], v1, %4, %0 offen sc1\n\t" \
    "v_exp_f32 v219, v219\n\t" \
    "v_exp_f32 v220, v220\n\t" \
    "v_exp_f32 v221, v221\n\t" \
    "v_exp_f32 v222, v222\n\t" \
    "v_smfmac_f32_32x32x32_bf16 v[228:243], v[92:95], v[212:219], v252\n\t" \
    "v_exp_f32 v223, v223\n\t" \
    "v_exp_f32 v224, v224\n\t" \
    "buffer_load_dwordx4 v[116:119], v1, %4, %1 offen sc1\n\t" \
    "v_exp_f32 v225, v225\n\t" \
    "v_exp_f32 v226, v226\n\t" \
    "v_exp_f32 v227, v227\n\t" \
    "v_exp_f32 v196, v196\n\t" \
    "v_exp_f32 v197, v197\n\t" \
    "v_smfmac_f32_32x32x32_bf16 v[228:243], v[96:99], v[220:227], v252\n\t" \
    "v_exp_f32 v198, v198\n\t" \
    "v_exp_f32 v199, v199\n\t" \
    "v_exp_f32 v200, v200\n\t" \
    "s_waitcnt vmcnt(3)\n\t" \
    "v_mfma_f32_32x32x16_bf16 v[212:227], v[24:27], v[244:247], 0\n\t" \
    "v_permlane16_swap_b32 v100, v104\n\t" \
    "v_permlane16_swap_b32 v101, v105\n\t" \
    "v_permlane16_swap_b32 v102, v106\n\t" \
    "v_permlane16_swap_b32 v103, v107\n\t" \
    "v_exp_f32 v201, v201\n\t" \
    "v_exp_f32 v202, v202\n\t" \
    "s_add_u32 %2, %9, 7\n\t" \
    "s_and_b32 %2, %2, 15\n\t" \
    "s_lshl_b32 %2, %2, 10\n\t" \
    "s_add_u32 %0, %2, %5\n\t" \
    "s_add_u32 %1, %2, %6\n\t" \
    "buffer_load_dwordx4 v[32:35], v1, %4, %0 offen sc1\n\t" \
    "v_exp_f32 v203, v203\n\t" \
    "v_exp_f32 v204, v204\n\t" \
    "v_exp_f32 v205, v205\n\t" \
    "v_exp_f32 v206, v206\n\t" \
    "v_smfmac_f32_32x32x32_bf16 v[228:243], v[100:103], v[196:203], v252\n\t" \
    "v_exp_f32 v207, v207\n\t" \
    "v_exp_f32 v208, v208\n\t" \
    "buffer_load_dwordx4 v[124:127], v1, %4, %1 offen sc1\n\t" \
    "v_exp_f32 v209, v209\n\t" \
    "v_exp_f32 v210, v210\n\t" \
    "v_exp_f32 v211, v211\n\t" \
    "v_exp_f32 v212, v212\n\t" \
    "v_exp_f32 v213, v213\n\t" \
    "v_smfmac_f32_32x32x32_bf16 v[228:243], v[104:107], v[204:211], v252\n\t" \
    "v_exp_f32 v214, v214\n\t" \
    "v_exp_f32 v215, v215\n\t" \
    "v_exp_f32 v216, v216\n\t" \
    "s_waitcnt vmcnt(3)\n\t" \
    "v_mfma_f32_32x32x16_bf16 v[196:211], v[28:31], v[244:247], 0\n\t" \
    "v_permlane16_swap_b32 v108, v112\n\t" \
    "v_permlane16_swap_b32 v109, v113\n\t" \
    "v_permlane16_swap_b32 v110, v114\n\t" \
    "v_permlane16_swap_b32 v111, v115\n\t" \
    "v_exp_f32 v217, v217\n\t" \
    "v_exp_f32 v218, v218\n\t" \
    "s_add_u32 %2, %9, 8\n\t" \
    "s_and_b32 %2, %2, 15\n\t" \
    "s_lshl_b32 %2, %2, 10\n\t" \
    "s_add_u32 %0, %2, %5\n\t" \
    "s_add_u32 %1, %2, %6\n\t" \
    "buffer_load_dwordx4 v[36:39], v1, %4, %0 offen sc1\n\t" \
    "v_exp_f32 v219, v219\n\t" \
    "v_exp_f32 v220, v220\n\t" \
    "v_exp_f32 v221, v221\n\t" \
    "v_exp_f32 v222, v222\n\t" \
    "v_smfmac_f32_32x32x32_bf16 v[228:243], v[108:111], v[212:219], v252\n\t" \
    "v_exp_f32 v223, v223\n\t" \
    "v_exp_f32 v224, v224\n\t" \
    "buffer_load_dwordx4 v[132:135], v1, %4, %1 offen sc1\n\t" \
    "v_exp_f32 v225, v225\n\t" \
    "v_exp_f32 v226, v226\n\t" \
    "v_exp_f32 v227, v227\n\t" \
    "v_exp_f32 v196, v196\n\t" \
    "v_exp_f32 v197, v197\n\t" \
    "v_smfmac_f32_32x32x32_bf16 v[228:243], v[112:115], v[220:227], v252\n\t" \
    "v_exp_f32 v198, v198\n\t" \
    "v_exp_f32 v199, v199\n\t" \
    "v_exp_f32 v200, v200\n\t" \
    "s_waitcnt vmcnt(3)\n\t" \
    "v_mfma_f32_32x32x16_bf16 v[212:227], v[32:35], v[244:247], 0\n\t" \
    "v_permlane16_swap_b32 v116, v120\n\t" \
    "v_permlane16_swap_b32 v117, v121\n\t" \
    "v_permlane16_swap_b32 v118, v122\n\t" \
    "v_permlane16_swap_b32 v119, v123\n\t" \
    "v_exp_f32 v201, v201\n\t" \
    "v_exp_f32 v202, v202\n\t" \
    "s_add_u32 %2, %9, 9\n\t" \
    "s_and_b32 %2, %2, 15\n\t" \
    "s_lshl_b32 %2, %2, 10\n\t" \
    "s_add_u32 %0, %2, %5\n\t" \
    "s_add_u32 %1, %2, %6\n\t" \
    "buffer_load_dwordx4 v[40:43], v1, %4, %0 offen sc1\n\t" \
    "v_exp_f32 v203, v203\n\t" \
    "v_exp_f32 v204, v204\n\t" \
    "v_exp_f32 v205, v205\n\t" \
    "v_exp_f32 v206, v206\n\t" \
    "v_smfmac_f32_32x32x32_bf16 v[228:243], v[116:119], v[196:203], v252\n\t" \
    "v_exp_f32 v207, v207\n\t" \
    "v_exp_f32 v208, v208\n\t" \
    "buffer_load_dwordx4 v[140:143], v1, %4, %1 offen sc1\n\t" \
    "v_exp_f32 v209, v209\n\t" \
    "v_exp_f32 v210, v210\n\t" \
    "v_exp_f32 v211, v211\n\t" \
    "v_exp_f32 v212, v212\n\t" \
    "v_exp_f32 v213, v213\n\t" \
    "v_smfmac_f32_32x32x32_bf16 v[228:243], v[120:123], v[204:211], v252\n\t" \
    "v_exp_f32 v214, v214\n\t" \
    "v_exp_f32 v215, v215\n\t" \
    "v_exp_f32 v216, v216\n\t" \
    "s_waitcnt vmcnt(3)\n\t" \
    "v_mfma_f32_32x32x16_bf16 v[196:211], v[36:39], v[244:247], 0\n\t" \
    "v_permlane16_swap_b32 v124, v128\n\t" \
    "v_permlane16_swap_b32 v125, v129\n\t" \
    "v_permlane16_swap_b32 v126, v130\n\t" \
    "v_permlane16_swap_b32 v127, v131\n\t" \
    "v_exp_f32 v217, v217\n\t" \
    "v_exp_f32 v218, v218\n\t" \
    "s_add_u32 %2, %9, 10\n\t" \
    "s_and_b32 %2, %2, 15\n\t" \
    "s_lshl_b32 %2, %2, 10\n\t" \
    "s_add_u32 %0, %2, %5\n\t" \
    "s_add_u32 %1, %2, %6\n\t" \
    "buffer_load_dwordx4 v[44:47], v1, %4, %0 offen sc1\n\t" \
    "v_exp_f32 v219, v219\n\t" \
    "v_exp_f32 v220, v220\n\t" \
    "v_exp_f32 v221, v221\n\t" \
    "v_exp_f32 v222, v222\n\t" \
    "v_smfmac_f32_32x32x32_bf16 v[228:243], v[124:127], v[212:219], v252\n\t" \
    "v_exp_f32 v223, v223\n\t" \
    "v_exp_f32 v224, v224\n\t" \
    "buffer_load_dwordx4 v[148:151], v1, %4, %1 offen sc1\n\t" \
    "v_exp_f32 v225, v225\n\t" \
    "v_exp_f32 v226, v226\n\t" \
    "v_exp_f32 v227, v227\n\t" \
    "v_exp_f32 v196, v196\n\t" \
    "v_exp_f32 v197, v197\n\t" \
    "v_smfmac_f32_32x32x32_bf16 v[228:243], v[128:131], v[220:227], v252\n\t" \
    "v_exp_f32 v198, v198\n\t" \
    "v_exp_f32 v199, v199\n\t" \
    "v_exp_f32 v200, v200\n\t" \
    "s_waitcnt vmcnt(3)\n\t" \
    "v_mfma_f32_32x32x16_bf16 v[212:227], v[40:43], v[244:247], 0\n\t" \
    "v_permlane16_swap_b32 v132, v136\n\t" \
    "v_permlane16_swap_b32 v133, v137\n\t" \
    "v_permlane16_swap_b32 v134, v138\n\t" \
    "v_permlane16_swap_b32 v135, v139\n\t" \
    "v_exp_f32 v201, v201\n\t" \
    "v_exp_f32 v202, v202\n\t" \
    "s_add_u32 %2, %9, 11\n\t" \
    "s_and_b32 %2, %2, 15\n\t" \
    "s_lshl_b32 %2, %2, 10\n\t" \
    "s_add_u32 %0, %2, %5\n\t" \
    "s_add_u32 %1, %2, %6\n\t" \
    "buffer_load_dwordx4 v[48:51], v1, %4, %0 offen sc1\n\t" \
    "v_exp_f32 v203, v203\n\t" \
    "v_exp_f32 v204, v204\n\t" \
    "v_exp_f32 v205, v205\n\t" \
    "v_exp_f32 v206, v206\n\t" \
    "v_smfmac_f32_32x32x32_bf16 v[228:243], v[132:135], v[196:203], v252\n\t" \
    "v_exp_f32 v207, v207\n\t" \
    "v_exp_f32 v208, v208\n\t" \
    "buffer_load_dwordx4 v[156:159], v1, %4, %1 offen sc1\n\t" \
    "v_exp_f32 v209, v209\n\t" \
    "v_exp_f32 v210, v210\n\t" \
    "v_exp_f32 v211, v211\n\t" \
    "v_exp_f32 v212, v212\n\t" \
    "v_exp_f32 v213, v213\n\t" \
    "v_smfmac_f32_32x32x32_bf16 v[228:243], v[136:139], v[204:211], v252\n\t" \
    "v_exp_f32 v214, v214\n\t" \
    "v_exp_f32 v215, v215\n\t" \
    "v_exp_f32 v216, v216\n\t" \
    "s_waitcnt vmcnt(3)\n\t" \
    "v_mfma_f32_32x32x16_bf16 v[196:211], v[44:47], v[244:247], 0\n\t" \
    "v_permlane16_swap_b32 v140, v144\n\t" \
    "v_permlane16_swap_b32 v141, v145\n\t" \
    "v_permlane16_swap_b32 v142, v146\n\t" \
    "v_permlane16_swap_b32 v143, v147\n\t" \
    "v_exp_f32 v217, v217\n\t" \
    "v_exp_f32 v218, v218\n\t" \
    "s_add_u32 %2, %9, 12\n\t" \
    "s_and_b32 %2, %2, 15\n\t" \
    "s_lshl_b32 %2, %2, 10\n\t" \
    "s_add_u32 %0, %2, %5\n\t" \
    "s_add_u32 %1, %2, %6\n\t" \
    "buffer_load_dwordx4 v[52:55], v1, %4, %0 offen sc1\n\t" \
    "v_exp_f32 v219, v219\n\t" \
    "v_exp_f32 v220, v220\n\t" \
    "v_exp_f32 v221, v221\n\t" \
    "v_exp_f32 v222, v222\n\t" \
    "v_smfmac_f32_32x32x32_bf16 v[228:243], v[140:143], v[212:219], v252\n\t" \
    "v_exp_f32 v223, v223\n\t" \
    "v_exp_f32 v224, v224\n\t" \
    "buffer_load_dwordx4 v[164:167], v1, %4, %1 offen sc1\n\t" \
    "v_exp_f32 v225, v225\n\t" \
    "v_exp_f32 v226, v226\n\t" \
    "v_exp_f32 v227, v227\n\t" \
    "v_exp_f32 v196, v196\n\t" \
    "v_exp_f32 v197, v197\n\t" \
    "v_smfmac_f32_32x32x32_bf16 v[228:243], v[144:147], v[220:227], v252\n\t" \
    "v_exp_f32 v198, v198\n\t" \
    "v_exp_f32 v199, v199\n\t" \
    "v_exp_f32 v200, v200\n\t" \
    "s_waitcnt vmcnt(3)\n\t" \
    "v_mfma_f32_32x32x16_bf16 v[212:227], v[48:51], v[244:247], 0\n\t" \
    "v_permlane16_swap_b32 v148, v152\n\t" \
    "v_permlane16_swap_b32 v149, v153\n\t" \
    "v_permlane16_swap_b32 v150, v154\n\t" \
    "v_permlane16_swap_b32 v151, v155\n\t" \
    "v_exp_f32 v201, v201\n\t" \
    "v_exp_f32 v202, v202\n\t" \
    "s_add_u32 %2, %9, 13\n\t" \
    "s_and_b32 %2, %2, 15\n\t" \
    "s_lshl_b32 %2, %2, 10\n\t" \
    "s_add_u32 %0, %2, %5\n\t" \
    "s_add_u32 %1, %2, %6\n\t" \
    "buffer_load_dwordx4 v[56:59], v1, %4, %0 offen sc1\n\t" \
    "v_exp_f32 v203, v203\n\t" \
    "v_exp_f32 v204, v204\n\t" \
    "v_exp_f32 v205, v205\n\t" \
    "v_exp_f32 v206, v206\n\t" \
    "v_smfmac_f32_32x32x32_bf16 v[228:243], v[148:151], v[196:203], v252\n\t" \
    "v_exp_f32 v207, v207\n\t" \
    "v_exp_f32 v208, v208\n\t" \
    "buffer_load_dwordx4 v[172:175], v1, %4, %1 offen sc1\n\t" \
    "v_exp_f32 v209, v209\n\t" \
    "v_exp_f32 v210, v210\n\t" \
    "v_exp_f32 v211, v211\n\t" \
    "v_exp_f32 v212, v212\n\t" \
    "v_exp_f32 v213, v213\n\t" \
    "v_smfmac_f32_32x32x32_bf16 v[228:243], v[152:155], v[204:211], v252\n\t" \
    "v_exp_f32 v214, v214\n\t" \
    "v_exp_f32 v215, v215\n\t" \
    "v_exp_f32 v216, v216\n\t" \
    "s_waitcnt vmcnt(3)\n\t" \
    "v_mfma_f32_32x32x16_bf16 v[196:211], v[52:55], v[244:247], 0\n\t" \
    "v_permlane16_swap_b32 v156, v160\n\t" \
    "v_permlane16_swap_b32 v157, v161\n\t" \
    "v_permlane16_swap_b32 v158, v162\n\t" \
    "v_permlane16_swap_b32 v159, v163\n\t" \
    "v_exp_f32 v217, v217\n\t" \
    "v_exp_f32 v218, v218\n\t" \
    "s_add_u32 %2, %9, 14\n\t" \
    "s_and_b32 %2, %2, 15\n\t" \
    "s_lshl_b32 %2, %2, 10\n\t" \
    "s_add_u32 %0, %2, %5\n\t" \
    "s_add_u32 %1, %2, %6\n\t" \
    "buffer_load_dwordx4 v[60:63], v1, %4, %0 offen sc1\n\t" \
    "v_exp_f32 v219, v219\n\t" \
    "v_exp_f32 v220, v220\n\t" \
    "v_exp_f32 v221, v221\n\t" \
    "v_exp_f32 v222, v222\n\t" \
    "v_smfmac_f32_32x32x32_bf16 v[228:243], v[156:159], v[212:219], v252\n\t" \
    "v_exp_f32 v223, v223\n\t" \
    "v_exp_f32 v224, v224\n\t" \
    "buffer_load_dwordx4 v[180:183], v1, %4, %1 offen sc1\n\t" \
    "v_exp_f32 v225, v225\n\t" \
    "v_exp_f32 v226, v226\n\t" \
    "v_exp_f32 v227, v227\n\t" \
    "v_exp_f32 v196, v196\n\t" \
    "v_exp_f32 v197, v197\n\t" \
    "v_smfmac_f32_32x32x32_bf16 v[228:243], v[160:163], v[220:227], v252\n\t" \
    "v_exp_f32 v198, v198\n\t" \
    "v_exp_f32 v199, v199\n\t" \
    "v_exp_f32 v200, v200\n\t" \
    "s_waitcnt vmcnt(3)\n\t" \
    "v_mfma_f32_32x32x16_bf16 v[212:227], v[56:59], v[244:247], 0\n\t" \
    "v_permlane16_swap_b32 v164, v168\n\t" \
    "v_permlane16_swap_b32 v165, v169\n\t" \
    "v_permlane16_swap_b32 v166, v170\n\t" \
    "v_permlane16_swap_b32 v167, v171\n\t" \
    "v_exp_f32 v201, v201\n\t" \
    "v_exp_f32 v202, v202\n\t" \
    "s_add_u32 %2, %9, 15\n\t" \
    "s_and_b32 %2, %2, 15\n\t" \
    "s_lshl_b32 %2, %2, 10\n\t" \
    "s_add_u32 %0, %2, %5\n\t" \
    "s_add_u32 %1, %2, %6\n\t" \
    "buffer_load_dwordx4 v[64:67], v1, %4, %0 offen sc1\n\t" \
    "v_exp_f32 v203, v203\n\t" \
    "v_exp_f32 v204, v204\n\t" \
    "v_exp_f32 v205, v205\n\t" \
    "v_exp_f32 v206, v206\n\t" \
    "v_smfmac_f32_32x32x32_bf16 v[228:243], v[164:167], v[196:203], v252\n\t" \
    "v_exp_f32 v207, v207\n\t" \
    "v_exp_f32 v208, v208\n\t" \
    "buffer_load_dwordx4 v[188:191], v1, %4, %1 offen sc1\n\t" \
    "v_exp_f32 v209, v209\n\t" \
    "v_exp_f32 v210, v210\n\t" \
    "v_exp_f32 v211, v211\n\t" \
    "v_exp_f32 v212, v212\n\t" \
    "v_exp_f32 v213, v213\n\t" \
    "v_smfmac_f32_32x32x32_bf16 v[228:243], v[168:171], v[204:211], v252\n\t" \
    "v_exp_f32 v214, v214\n\t" \
    "v_exp_f32 v215, v215\n\t" \
    "v_exp_f32 v216, v216\n\t" \
    "s_waitcnt vmcnt(3)\n\t" \
    "v_mfma_f32_32x32x16_bf16 v[196:211], v[60:63], v[244:247], 0\n\t" \
    "v_permlane16_swap_b32 v172, v176\n\t" \
    "v_permlane16_swap_b32 v173, v177\n\t" \
    "v_permlane16_swap_b32 v174, v178\n\t" \
    "v_permlane16_swap_b32 v175, v179\n\t" \
    "v_exp_f32 v217, v217\n\t" \
    "v_exp_f32 v218, v218\n\t" \
    "v_exp_f32 v219, v219\n\t" \
    "v_exp_f32 v220, v220\n\t" \
    "v_exp_f32 v221, v221\n\t" \
    "v_exp_f32 v222, v222\n\t" \
    "v_smfmac_f32_32x32x32_bf16 v[228:243], v[172:175], v[212:219], v252\n\t" \
    "v_exp_f32 v223, v223\n\t" \
    "v_exp_f32 v224, v224\n\t" \
    "v_exp_f32 v225, v225\n\t" \
    "v_exp_f32 v226, v226\n\t" \
    "v_exp_f32 v227, v227\n\t" \
    "v_exp_f32 v196, v196\n\t" \
    "v_exp_f32 v197, v197\n\t" \
    "v_smfmac_f32_32x32x32_bf16 v[228:243], v[176:179], v[220:227], v252\n\t" \
    "v_exp_f32 v198, v198\n\t" \
    "v_exp_f32 v199, v199\n\t" \
    "v_exp_f32 v200, v200\n\t" \
    "s_waitcnt vmcnt(1)\n\t" \
    "v_mfma_f32_32x32x16_bf16 v[212:227], v[64:67], v[244:247], 0\n\t" \
    "v_permlane16_swap_b32 v180, v184\n\t" \
    "v_permlane16_swap_b32 v181, v185\n\t" \
    "v_permlane16_swap_b32 v182, v186\n\t" \
    "v_permlane16_swap_b32 v183, v187\n\t" \
    "v_add_u32 v253, 1024, v253\n\t" \
    "v_exp_f32 v201, v201\n\t" \
    "ds_read_b128 v[244:247], v253\n\t" \
    "v_exp_f32 v202, v202\n\t" \
    "v_exp_f32 v203, v203\n\t" \
    "v_exp_f32 v204, v204\n\t" \
    "v_exp_f32 v205, v205\n\t" \
    "v_exp_f32 v206, v206\n\t" \
    "v_smfmac_f32_32x32x32_bf16 v[228:243], v[180:183], v[196:203], v252\n\t" \
    "v_exp_f32 v207, v207\n\t" \
    "v_exp_f32 v208, v208\n\t" \
    "v_exp_f32 v209, v209\n\t" \
    "v_exp_f32 v210, v210\n\t" \
    "v_exp_f32 v211, v211\n\t" \
    "v_exp_f32 v212, v212\n\t" \
    "v_exp_f32 v213, v213\n\t" \
    "v_smfmac_f32_32x32x32_bf16 v[228:243], v[184:187], v[204:211], v252\n\t" \
    "v_exp_f32 v214, v214\n\t" \
    "v_exp_f32 v215, v215\n\t" \
    "v_exp_f32 v216, v216\n\t" \
    "s_waitcnt lgkmcnt(0)\n\t" \
    "s_waitcnt vmcnt(0)\n\t" \
    "v_mfma_f32_32x32x16_bf16 v[196:211], v[4:7], v[244:247], 0\n\t" \
    "v_permlane16_swap_b32 v188, v192\n\t" \
    "v_permlane16_swap_b32 v189, v193\n\t" \
    "v_permlane16_swap_b32 v190, v194\n\t" \
    "v_permlane16_swap_b32 v191, v195\n\t" \
    "v_exp_f32 v217, v217\n\t" \
    "v_exp_f32 v218, v218\n\t" \
    "v_exp_f32 v219, v219\n\t" \
    "v_exp_f32 v220, v220\n\t" \
    "v_exp_f32 v221, v221\n\t" \
    "v_exp_f32 v222, v222\n\t" \
    "v_smfmac_f32_32x32x32_bf16 v[228:243], v[188:191], v[212:219], v252\n\t" \
    "v_exp_f32 v223, v223\n\t" \
    "v_exp_f32 v224, v224\n\t" \
    "v_exp_f32 v225, v225\n\t" \
    "v_exp_f32 v226, v226\n\t" \
    "v_exp_f32 v227, v227\n\t" \
    "s_mov_b32 %1, 1\n\t" \
    "2:\n\t" \
    "s_mov_b32 %0, 9216\n\t" \
    "s_cmp_eq_u32 %1, 6\n\t" \
    "s_cselect_b32 %0, 4294813696, %0\n\t" \
    "s_cmp_eq_u32 %1, 3\n\t" \
    "s_cbranch_scc0 30f\n\t" \
    "s_add_u32 s64, %7, 32\n\t" \
    "s_mov_b32 %2, 0\n\t" \
    "3:\n\t" \
    "v_mov_b32 v2, s64\n\t" \
    "ds_read_b32 v2, v2\n\t" \
    "s_waitcnt lgkmcnt(0)\n\t" \
    "v_readfirstlane_b32 vcc_lo, v2\n\t" \
    "s_add_u32 %2, %2, 1\n\t" \
    "s_cmp_ge_u32 vcc_lo, 4\n\t" \
    "s_cbranch_scc1 30f\n\t" \
    "s_sleep 1\n\t" \
    "s_cmp_lt_u32 %2, 0x400000\n\t" \
    "s_cbranch_scc1 3b\n\t" \
    "30:\n\t" \
    "v_exp_f32 v196, v196\n\t" \
    "v_exp_f32 v197, v197\n\t" \
    "v_smfmac_f32_32x32x32_bf16 v[228:243], v[192:195], v[220:227], v252\n\t" \
    "v_exp_f32 v198, v198\n\t" \
    "v_exp_f32 v199, v199\n\t" \
    "v_exp_f32 v200, v200\n\t" \
    "v_mfma_f32_32x32x16_bf16 v[212:227], v[8:11], v[244:247], 0\n\t" \
    "v_exp_f32 v201, v201\n\t" \
    "v_exp_f32 v202, v202\n\t" \
    "v_exp_f32 v203, v203\n\t" \
    "v_exp_f32 v204, v204\n\t" \
    "v_exp_f32 v205, v205\n\t" \
    "v_exp_f32 v206, v206\n\t" \
    "v_exp_f32 v207, v207\n\t" \
    "v_exp_f32 v208, v208\n\t" \
    "v_exp_f32 v209, v209\n\t" \
    "v_add_u32 v0, 1, v0\n\t" \
    "ds_write_b128 v254, v[228:231]\n\t" \
    "ds_write_b128 v254, v[232:235] offset:1024\n\t" \
    "ds_write_b32 v3, v236 offset:2048\n\t" \
    "ds_write_b32 v255, v0\n\t" \
    "v_add_u32 v254, %0, v254\n\t" \
    "v_add_u32 v3, %0, v3\n\t" \
    "v_exp_f32 v210, v210\n\t" \
    "v_mfma_f32_32x32x16_bf16 v[228:243], v[248:251], v[248:251], 0\n\t" \
    "v_exp_f32 v211, v211\n\t" \
    "v_smfmac_f32_32x32x32_bf16 v[228:243], v[68:71], v[196:203], v252\n\t" \
    "v_exp_f32 v212, v212\n\t" \
    "v_exp_f32 v213, v213\n\t" \
    "v_smfmac_f32_32x32x32_bf16 v[228:243], v[72:75], v[204:211], v252\n\t" \
    "v_exp_f32 v214, v214\n\t" \
    "v_exp_f32 v215, v215\n\t" \
    "v_exp_f32 v216, v216\n\t" \
    "v_mfma_f32_32x32x16_bf16 v[196:211], v[12:15], v[244:247], 0\n\t" \
    "v_exp_f32 v217, v217\n\t" \
    "v_exp_f32 v218, v218\n\t" \
    "v_exp_f32 v219, v219\n\t" \
    "v_exp_f32 v220, v220\n\t" \
    "v_exp_f32 v221, v221\n\t" \
    "v_exp_f32 v222, v222\n\t" \
    "v_smfmac_f32_32x32x32_bf16 v[228:243], v[76:79], v[212:219], v252\n\t" \
    "v_exp_f32 v223, v223\n\t" \
    "v_exp_f32 v224, v224\n\t" \
    "v_exp_f32 v225, v225\n\t" \
    "v_exp_f32 v226, v226\n\t" \
    "v_exp_f32 v227, v227\n\t" \
    "v_exp_f32 v196, v196\n\t" \
    "v_exp_f32 v197, v197\n\t" \
    "v_smfmac_f32_32x32x32_bf16 v[228:243], v[80:83], v[220:227], v252\n\t" \
    "v_exp_f32 v198, v198\n\t" \
    "v_exp_f32 v199, v199\n\t" \
    "v_exp_f32 v200, v200\n\t" \
    "v_mfma_f32_32x32x16_bf16 v[212:227], v[16:19], v[244:247], 0\n\t" \
    "v_exp_f32 v201, v201\n\t" \
    "v_exp_f32 v202, v202\n\t" \
    "v_exp_f32 v203, v203\n\t" \
    "v_exp_f32 v204, v204\n\t" \
    "v_exp_f32 v205, v205\n\t" \
    "v_exp_f32 v206, v206\n\t" \
    "v_smfmac_f32_32x32x32_bf16 v[228:243], v[84:87], v[196:203], v252\n\t" \
    "v_exp_f32 v207, v207\n\t" \
    "v_exp_f32 v208, v208\n\t" \
    "v_exp_f32 v209, v209\n\t" \
    "v_exp_f32 v210, v210\n\t" \
    "v_exp_f32 v211, v211\n\t" \
    "v_exp_f32 v212, v212\n\t" \
    "v_exp_f32 v213, v213\n\t" \
    "v_smfmac_f32_32x32x32_bf16 v[228:243], v[88:91], v[204:211], v252\n\t" \
    "v_exp_f32 v214, v214\n\t" \
    "v_exp_f32 v215, v215\n\t" \
    "v_exp_f32 v216, v216\n\t" \
    "v_mfma_f32_32x32x16_bf16 v[196:211], v[20:23], v[244:247], 0\n\t" \
    "v_exp_f32 v217, v217\n\t" \
    "v_exp_f32 v218, v218\n\t" \
    "v_exp_f32 v219, v219\n\t" \
    "v_exp_f32 v220, v220\n\t" \
    "v_exp_f32 v221, v221\n\t" \
    "v_exp_f32 v222, v222\n\t" \
    "v_smfmac_f32_32x32x32_bf16 v[228:243], v[92:95], v[212:219], v252\n\t" \
    "v_exp_f32 v223, v223\n\t" \
    "v_exp_f32 v224, v224\n\t" \
    "v_exp_f32 v225, v225\n\t" \
    "v_exp_f32 v226, v226\n\t" \
    "v_exp_f32 v227, v227\n\t" \
    "v_exp_f32 v196, v196\n\t" \
    "v_exp_f32 v197, v197\n\t" \
    "v_smfmac_f32_32x32x32_bf16 v[228:243], v[96:99], v[220:227], v252\n\t" \
    "v_exp_f32 v198, v198\n\t" \
    "v_exp_f32 v199, v199\n\t" \
    "v_exp_f32 v200, v200\n\t" \
    "v_mfma_f32_32x32x16_bf16 v[212:227], v[24:27], v[244:247], 0\n\t" \
    "v_exp_f32 v201, v201\n\t" \
    "v_exp_f32 v202, v202\n\t" \
    "v_exp_f32 v203, v203\n\t" \
    "v_exp_f32 v204, v204\n\t" \
    "v_exp_f32 v205, v205\n\t" \
    "v_exp_f32 v206, v206\n\t" \
    "v_smfmac_f32_32x32x32_bf16 v[228:243], v[100:103], v[196:203], v252\n\t" \
    "v_exp_f32 v207, v207\n\t" \
    "v_exp_f32 v208, v208\n\t" \
    "v_exp_f32 v209, v209\n\t" \
    "v_exp_f32 v210, v210\n\t" \
    "v_exp_f32 v211, v211\n\t" \
    "v_exp_f32 v212, v212\n\t" \
    "v_exp_f32 v213, v213\n\t" \
    "v_smfmac_f32_32x32x32_bf16 v[228:243], v[104:107], v[204:211], v252\n\t" \
    "v_exp_f32 v214, v214\n\t" \
    "v_exp_f32 v215, v215\n\t" \
    "v_exp_f32 v216, v216\n\t" \
    "v_mfma_f32_32x32x16_bf16 v[196:211], v[28:31], v[244:247], 0\n\t" \
    "v_exp_f32 v217, v217\n\t" \
    "v_exp_f32 v218, v218\n\t" \
    "v_exp_f32 v219, v219\n\t" \
    "v_exp_f32 v220, v220\n\t" \
    "v_exp_f32 v221, v221\n\t" \
    "v_exp_f32 v222, v222\n\t" \
    "v_smfmac_f32_32x32x32_bf16 v[228:243], v[108:111], v[212:219], v252\n\t" \
    "v_exp_f32 v223, v223\n\t" \
    "v_exp_f32 v224, v224\n\t" \
    "v_exp_f32 v225, v225\n\t" \
    "v_exp_f32 v226, v226\n\t" \
    "v_exp_f32 v227, v227\n\t" \
    "v_exp_f32 v196, v196\n\t" \
    "v_exp_f32 v197, v197\n\t" \
    "v_smfmac_f32_32x32x32_bf16 v[228:243], v[112:115], v[220:227], v252\n\t" \
    "v_exp_f32 v198, v198\n\t" \
    "v_exp_f32 v199, v199\n\t" \
    "v_exp_f32 v200, v200\n\t" \
    "v_mfma_f32_32x32x16_bf16 v[212:227], v[32:35], v[244:247], 0\n\t" \
    "v_exp_f32 v201, v201\n\t" \
    "v_exp_f32 v202, v202\n\t" \
    "v_exp_f32 v203, v203\n\t" \
    "v_exp_f32 v204, v204\n\t" \
    "v_exp_f32 v205, v205\n\t" \
    "v_exp_f32 v206, v206\n\t" \
    "v_smfmac_f32_32x32x32_bf16 v[228:243], v[116:119], v[196:203], v252\n\t" \
    "v_exp_f32 v207, v207\n\t" \
    "v_exp_f32 v208, v208\n\t" \
    "v_exp_f32 v209, v209\n\t" \
    "v_exp_f32 v210, v210\n\t" \
    "v_exp_f32 v211, v211\n\t" \
    "v_exp_f32 v212, v212\n\t" \
    "v_exp_f32 v213, v213\n\t" \
    "v_smfmac_f32_32x32x32_bf16 v[228:243], v[120:123], v[204:211], v252\n\t" \
    "v_exp_f32 v214, v214\n\t" \
    "v_exp_f32 v215, v215\n\t" \
    "v_exp_f32 v216, v216\n\t" \
    "v_mfma_f32_32x32x16_bf16 v[196:211], v[36:39], v[244:247], 0\n\t" \
    "v_exp_f32 v217, v217\n\t" \
    "v_exp_f32 v218, v218\n\t" \
    "v_exp_f32 v219, v219\n\t" \
    "v_exp_f32 v220, v220\n\t" \
    "v_exp_f32 v221, v221\n\t" \
    "v_exp_f32 v222, v222\n\t" \
    "v_smfmac_f32_32x32x32_bf16 v[228:243], v[124:127], v[212:219], v252\n\t" \
    "v_exp_f32 v223, v223\n\t" \
    "v_exp_f32 v224, v224\n\t" \
    "v_exp_f32 v225, v225\n\t" \
    "v_exp_f32 v226, v226\n\t" \
    "v_exp_f32 v227, v227\n\t" \
    "v_exp_f32 v196, v196\n\t" \
    "v_exp_f32 v197, v197\n\t" \
    "v_smfmac_f32_32x32x32_bf16 v[228:243], v[128:131], v[220:227], v252\n\t" \
    "v_exp_f32 v198, v198\n\t" \
    "v_exp_f32 v199, v199\n\t" \
    "v_exp_f32 v200, v200\n\t" \
    "v_mfma_f32_32x32x16_bf16 v[212:227], v[40:43], v[244:247], 0\n\t" \
    "v_exp_f32 v201, v201\n\t" \
    "v_exp_f32 v202, v202\n\t" \
    "v_exp_f32 v203, v203\n\t" \
    "v_exp_f32 v204, v204\n\t" \
    "v_exp_f32 v205, v205\n\t" \
    "v_exp_f32 v206, v206\n\t" \
    "v_smfmac_f32_32x32x32_bf16 v[228:243], v[132:135], v[196:203], v252\n\t" \
    "v_exp_f32 v207, v207\n\t" \
    "v_exp_f32 v208, v208\n\t" \
    "v_exp_f32 v209, v209\n\t" \
    "v_exp_f32 v210, v210\n\t" \
    "v_exp_f32 v211, v211\n\t" \
    "v_exp_f32 v212, v212\n\t" \
    "v_exp_f32 v213, v213\n\t" \
    "v_smfmac_f32_32x32x32_bf16 v[228:243], v[136:139], v[204:211], v252\n\t" \
    "v_exp_f32 v214, v214\n\t" \
    "v_exp_f32 v215, v215\n\t" \
    "v_exp_f32 v216, v216\n\t" \
    "v_mfma_f32_32x32x16_bf16 v[196:211], v[44:47], v[244:247], 0\n\t" \
    "v_exp_f32 v217, v217\n\t" \
    "v_exp_f32 v218, v218\n\t" \
    "v_exp_f32 v219, v219\n\t" \
    "v_exp_f32 v220, v220\n\t" \
    "v_exp_f32 v221, v221\n\t" \
    "v_exp_f32 v222, v222\n\t" \
    "v_smfmac_f32_32x32x32_bf16 v[228:243], v[140:143], v[212:219], v252\n\t" \
    "v_exp_f32 v223, v223\n\t" \
    "v_exp_f32 v224, v224\n\t" \
    "v_exp_f32 v225, v225\n\t" \
    "v_exp_f32 v226, v226\n\t" \
    "v_exp_f32 v227, v227\n\t" \
    "v_exp_f32 v196, v196\n\t" \
    "v_exp_f32 v197, v197\n\t" \
    "v_smfmac_f32_32x32x32_bf16 v[228:243], v[144:147], v[220:227], v252\n\t" \
    "v_exp_f32 v198, v198\n\t" \
    "v_exp_f32 v199, v199\n\t" \
    "v_exp_f32 v200, v200\n\t" \
    "v_mfma_f32_32x32x16_bf16 v[212:227], v[48:51], v[244:247], 0\n\t" \
    "v_exp_f32 v201, v201\n\t" \
    "v_exp_f32 v202, v202\n\t" \
    "v_exp_f32 v203, v203\n\t" \
    "v_exp_f32 v204, v204\n\t" \
    "v_exp_f32 v205, v205\n\t" \
    "v_exp_f32 v206, v206\n\t" \
    "v_smfmac_f32_32x32x32_bf16 v[228:243], v[148:151], v[196:203], v252\n\t" \
    "v_exp_f32 v207, v207\n\t" \
    "v_exp_f32 v208, v208\n\t" \
    "v_exp_f32 v209, v209\n\t" \
    "v_exp_f32 v210, v210\n\t" \
    "v_exp_f32 v211, v211\n\t" \
    "v_exp_f32 v212, v212\n\t" \
    "v_exp_f32 v213, v213\n\t" \
    "v_smfmac_f32_32x32x32_bf16 v[228:243], v[152:155], v[204:211], v252\n\t" \
    "v_exp_f32 v214, v214\n\t" \
    "v_exp_f32 v215, v215\n\t" \
    "v_exp_f32 v216, v216\n\t" \
    "v_mfma_f32_32x32x16_bf16 v[196:211], v[52:55], v[244:247], 0\n\t" \
    "v_exp_f32 v217, v217\n\t" \
    "v_exp_f32 v218, v218\n\t" \
    "v_exp_f32 v219, v219\n\t" \
    "v_exp_f32 v220, v220\n\t" \
    "v_exp_f32 v221, v221\n\t" \
    "v_exp_f32 v222, v222\n\t" \
    "v_smfmac_f32_32x32x32_bf16 v[228:243], v[156:159], v[212:219], v252\n\t" \
    "v_exp_f32 v223, v223\n\t" \
    "v_exp_f32 v224, v224\n\t" \
    "v_exp_f32 v225, v225\n\t" \
    "v_exp_f32 v226, v226\n\t" \
    "v_exp_f32 v227, v227\n\t" \
    "v_exp_f32 v196, v196\n\t" \
    "v_exp_f32 v197, v197\n\t" \
    "v_smfmac_f32_32x32x32_bf16 v[228:243], v[160:163], v[220:227], v252\n\t" \
    "v_exp_f32 v198, v198\n\t" \
    "v_exp_f32 v199, v199\n\t" \
    "v_exp_f32 v200, v200\n\t" \
    "v_mfma_f32_32x32x16_bf16 v[212:227], v[56:59], v[244:247], 0\n\t" \
    "v_exp_f32 v201, v201\n\t" \
    "v_exp_f32 v202, v202\n\t" \
    "v_exp_f32 v203, v203\n\t" \
    "v_exp_f32 v204, v204\n\t" \
    "v_exp_f32 v205, v205\n\t" \
    "v_exp_f32 v206, v206\n\t" \
    "v_smfmac_f32_32x32x32_bf16 v[228:243], v[164:167], v[196:203], v252\n\t" \
    "v_exp_f32 v207, v207\n\t" \
    "v_exp_f32 v208, v208\n\t" \
    "v_exp_f32 v209, v209\n\t" \
    "v_exp_f32 v210, v210\n\t" \
    "v_exp_f32 v211, v211\n\t" \
    "v_exp_f32 v212, v212\n\t" \
    "v_exp_f32 v213, v213\n\t" \
    "v_smfmac_f32_32x32x32_bf16 v[228:243], v[168:171], v[204:211], v252\n\t" \
    "v_exp_f32 v214, v214\n\t" \
    "v_exp_f32 v215, v215\n\t" \
    "v_exp_f32 v216, v216\n\t" \
    "v_mfma_f32_32x32x16_bf16 v[196:211], v[60:63], v[244:247], 0\n\t" \
    "v_exp_f32 v217, v217\n\t" \
    "v_exp_f32 v218, v218\n\t" \
    "v_exp_f32 v219, v219\n\t" \
    "v_exp_f32 v220, v220\n\t" \
    "v_exp_f32 v221, v221\n\t" \
    "v_exp_f32 v222, v222\n\t" \
    "v_smfmac_f32_32x32x32_bf16 v[228:243], v[172:175], v[212:219], v252\n\t" \
    "v_exp_f32 v223, v223\n\t" \
    "v_exp_f32 v224, v224\n\t" \
    "v_exp_f32 v225, v225\n\t" \
    "v_exp_f32 v226, v226\n\t" \
    "v_exp_f32 v227, v227\n\t" \
    "v_exp_f32 v196, v196\n\t" \
    "v_exp_f32 v197, v197\n\t" \
    "v_smfmac_f32_32x32x32_bf16 v[228:243], v[176:179], v[220:227], v252\n\t" \
    "v_exp_f32 v198, v198\n\t" \
    "v_exp_f32 v199, v199\n\t" \
    "v_exp_f32 v200, v200\n\t" \
    "v_mfma_f32_32x32x16_bf16 v[212:227], v[64:67], v[244:247], 0\n\t" \
    "v_add_u32 v253, 1024, v253\n\t" \
    "v_exp_f32 v201, v201\n\t" \
    "ds_read_b128 v[244:247], v253\n\t" \
    "v_exp_f32 v202, v202\n\t" \
    "v_exp_f32 v203, v203\n\t" \
    "v_exp_f32 v204, v204\n\t" \
    "v_exp_f32 v205, v205\n\t" \
    "v_exp_f32 v206, v206\n\t" \
    "v_smfmac_f32_32x32x32_bf16 v[228:243], v[180:183], v[196:203], v252\n\t" \
    "v_exp_f32 v207, v207\n\t" \
    "v_exp_f32 v208, v208\n\t" \
    "v_exp_f32 v209, v209\n\t" \
    "v_exp_f32 v210, v210\n\t" \
    "v_exp_f32 v211, v211\n\t" \
    "v_exp_f32 v212, v212\n\t" \
    "v_exp_f32 v213, v213\n\t" \
    "v_smfmac_f32_32x32x32_bf16 v[228:243], v[184:187], v[204:211], v252\n\t" \
    "v_exp_f32 v214, v214\n\t" \
    "v_exp_f32 v215, v215\n\t" \
    "v_exp_f32 v216, v216\n\t" \
    "s_waitcnt lgkmcnt(0)\n\t" \
    "v_mfma_f32_32x32x16_bf16 v[196:211], v[4:7], v[244:247], 0\n\t" \
    "v_exp_f32 v217, v217\n\t" \
    "v_exp_f32 v218, v218\n\t" \
    "v_exp_f32 v219, v219\n\t" \
    "v_exp_f32 v220, v220\n\t" \
    "v_exp_f32 v221, v221\n\t" \
    "v_exp_f32 v222, v222\n\t" \
    "v_smfmac_f32_32x32x32_bf16 v[228:243], v[188:191], v[212:219], v252\n\t" \
    "v_exp_f32 v223, v223\n\t" \
    "v_exp_f32 v224, v224\n\t" \
    "v_exp_f32 v225, v225\n\t" \
    "v_exp_f32 v226, v226\n\t" \
    "v_exp_f32 v227, v227\n\t" \
    "s_add_u32 %1, %1, 1\n\t" \
    "s_cmp_lt_u32 %1, 8\n\t" \
    "s_cbranch_scc1 2b\n\t" \
    "s_nop 1\n\t" \
    "v_smfmac_f32_32x32x32_bf16 v[228:243], v[192:195], v[220:227], v252\n\t" \
    "v_mbcnt_lo_u32_b32 v6, -1, 0\n\t" \
    "v_mbcnt_hi_u32_b32 v6, -1, v6\n\t" \
    "s_sub_u32 %1, %7, 153856\n\t" \
    "v_lshlrev_b32 v7, 4, v6\n\t" \
    "v_and_b32 v6, 31, v6\n\t" \
    "v_add_u32 v8, %1, v7\n\t" \
    "v_lshl_add_u32 v6, v6, 2, %1\n\t" \
    "v_mov_b32 v9, %7\n\t" \
    "s_nop 5\n\t" \
    "v_add_u32 v0, 1, v0\n\t" \
    "ds_write_b128 v254, v[228:231]\n\t" \
    "ds_write_b128 v254, v[232:235] offset:1024\n\t" \
    "ds_write_b32 v3, v236 offset:2048\n\t" \
    "ds_write_b32 v255, v0\n\t" \
    "v_add_u32 v254, %0, v254\n\t" \
    "v_add_u32 v3, %0, v3\n\t" \
    "s_add_u32 %0, %7, 80\n\t" \
    "v_mov_b32 v4, %0\n\t" \
    "v_mov_b32 v5, 1\n\t" \
    "s_mov_b64 exec, 1\n\t" \
    "ds_add_rtn_u32 v5, v4, v5\n\t" \
    "s_mov_b64 exec, -1\n\t" \
    "ds_read_b32 v10, v6 offset:2048\n\t" \
    "ds_read_b32 v11, v6 offset:4352\n\t" \
    "ds_read_b32 v12, v6 offset:6656\n\t" \
    "ds_read_b32 v13, v6 offset:8960\n\t" \
    "ds_read_b32 v49, v9 offset:256\n\t" \
    "ds_read_b128 v[16:19], v8 offset:0\n\t" \
    "ds_read_b128 v[20:23], v8 offset:1024\n\t" \
    "ds_read_b128 v[24:27], v8 offset:2304\n\t" \
    "ds_read_b128 v[28:31], v8 offset:3328\n\t" \
    "ds_read_b128 v[32:35], v8 offset:4608\n\t" \
    "ds_read_b128 v[36:39], v8 offset:5632\n\t" \
    "ds_read_b128 v[40:43], v8 offset:6912\n\t" \
    "ds_read_b128 v[44:47], v8 offset:7936\n\t" \
    "s_waitcnt lgkmcnt(13)\n\t" \
    "v_readfirstlane_b32 %0, v5\n\t" \
    "s_cmp_eq_u32 %0, 3\n\t" \
    "s_mov_b32 %0, 0\n\t" \
    "s_cbranch_scc0 15f\n\t" \
    "s_mov_b32 s64, 0x17800000\n\t" \
    "s_mov_b32 s65, 0x67800000\n\t" \
    "s_waitcnt lgkmcnt(8)\n\t" \
    "v_add_f32 v10, v10, v11\n\t" \
    "v_add_f32 v12, v12, v13\n\t" \
    "v_mov_b32 v51, v49\n\t" \
    "v_add_f32 v10, v10, v12\n\t" \
    "v_mov_b32 v53, v49\n\t" \
    "v_mov_b32 v55, v49\n\t" \
    "v_cmp_le_f32 vcc, s64, v10\n\t" \
    "v_cmp_ge_f32 s[64:65], s65, v10\n\t" \
    "v_rcp_f32 v10, v10\n\t" \
    "s_and_b64 vcc, vcc, s[64:65]\n\t" \
    "s_mov_b32 %0, 1\n\t" \
    "s_cmp_eq_u64 vcc, -1\n\t" \
    "s_cbranch_scc0 15f\n\t" \
    "s_mov_b32 %0, 0\n\t" \
    "s_waitcnt lgkmcnt(4)\n\t" \
    "v_pk_add_f32 v[16:17], v[16:17], v[24:25]\n\t" \
    "v_pk_add_f32 v[18:19], v[18:19], v[26:27]\n\t" \
    "v_pk_add_f32 v[20:21], v[20:21], v[28:29]\n\t" \
    "v_pk_add_f32 v[22:23], v[22:23], v[30:31]\n\t" \
    "s_waitcnt lgkmcnt(0)\n\t" \
    "v_pk_add_f32 v[32:33], v[32:33], v[40:41]\n\t" \
    "v_pk_add_f32 v[34:35], v[34:35], v[42:43]\n\t" \
    "v_pk_add_f32 v[36:37], v[36:37], v[44:45]\n\t" \
    "v_pk_add_f32 v[38:39], v[38:39], v[46:47]\n\t" \
    "v_pk_add_f32 v[16:17], v[16:17], v[32:33]\n\t" \
    "v_pk_add_f32 v[18:19], v[18:19], v[34:35]\n\t" \
    "v_pk_add_f32 v[20:21], v[20:21], v[36:37]\n\t" \
    "v_pk_add_f32 v[22:23], v[22:23], v[38:39]\n\t" \
    "v_mul_f32 v16, v10, v16\n\t" \
    "v_mul_f32 v17, v10, v17\n\t" \
    "v_mul_f32 v18, v10, v18\n\t" \
    "v_mul_f32 v19, v10, v19\n\t" \
    "v_mul_f32 v20, v10, v20\n\t" \
    "v_mul_f32 v21, v10, v21\n\t" \
    "v_mul_f32 v22, v10, v22\n\t" \
    "v_mul_f32 v23, v10, v23\n\t" \
    "v_cvt_pk_bf16_f32 v48, v16, v17\n\t" \
    "v_cvt_pk_bf16_f32 v50, v18, v19\n\t" \
    "v_cvt_pk_bf16_f32 v52, v20, v21\n\t" \
    "v_cvt_pk_bf16_f32 v54, v22, v23\n\t" \
    "buffer_store_dwordx4 v[48:51], v7, %4, %10 offen sc1\n\t" \
    "buffer_store_dwordx4 v[52:55], v7, %4, %10 offen offset:1024 sc1\n\t" \
    "15:\n\t" \
    "s_waitcnt lgkmcnt(0)\n\t" \
    ".Lcode_tail:\n\t"
#define CLOB_ATTN "v4","v5","v6","v7","v8","v9","v10","v11","v12","v13","v14","v15","v16","v17","v18","v19","v20","v21","v22","v23","v24","v25","v26","v27","v28","v29","v30","v31","v32","v33","v34","v35","v36","v37","v38","v39","v40","v41","v42","v43","v44","v45","v46","v47","v48","v49","v50","v51","v52","v53","v54","v55","v56","v57","v58","v59","v60","v61","v62","v63","v64","v65","v66","v67","v68","v69","v70","v71","v72","v73","v74","v75","v76","v77","v78","v79","v80","v81","v82","v83","v84","v85","v86","v87","v88","v89","v90","v91","v92","v93","v94","v95","v96","v97","v98","v99","v100","v101","v102","v103","v104","v105","v106","v107","v108","v109","v110","v111","v112","v113","v114","v115","v116","v117","v118","v119","v120","v121","v122","v123","v124","v125","v126","v127","v128","v129","v130","v131","v132","v133","v134","v135","v136","v137","v138","v139","v140","v141","v142","v143","v144","v145","v146","v147","v148","v149","v150","v151","v152","v153","v154","v155","v156","v157","v158","v159","v160","v161","v162","v163","v164","v165","v166","v167","v168","v169","v170","v171","v172","v173","v174","v175","v176","v177","v178","v179","v180","v181","v182","v183","v184","v185","v186","v187","v188","v189","v190","v191","v192","v193","v194","v195","v202","v203","v204","v205","v206","v207","v208","v209","v210","v211","v212","v213","v214","v215","v216","v217","v218","v219","v220","v221","v222","v223","v224","v225","v226","v227","v228","v229","v230","v231","v232","v233","v234","v235","v236","v237","v238","v239","v240","v241","v242","v243","v244","v245","v246","v247","v248","v249","v250","v251","v252","v0"
#define NE 3
#define TRY3 1

constexpr u32 WS_E = 0, WS_KVF = 32768, WS_HF = 0x240000, WS_OF = 0x280000, WS_K = 65536, WS_V = 131072, WS_O = 262144,
              WS_T = 3145728, WS_H = 4194304, WS_END = WS_H + 32 * 112 * 2048;
constexpr u32 XROW = 1040, L_XN = 0, L_P6 = 0, L_R2 = 27648, L_WKV = 66560, L_GR = 66560, L_WQL = 95232,
              L_WQ = 99328, L_Q = 99328, L_P0 = 107520, PSLOT = 2304, L_UX = L_P0 + 8 * PSLOT, L_CTL = 162816, L_RSTD = L_CTL,
              C_LN = L_CTL + 256, C_KV = L_CTL + 260, C_WQ = L_CTL + 264, C_W2 = L_CTL + 268, C_RS = L_CTL + 272, C_Q4 = L_CTL + 276, C_QD = L_CTL + 280,
              C_Q = L_CTL + 284, C_H = L_CTL + 288, C_R1 = L_CTL + 292, C_R2 = L_CTL + 296, C_OWN = L_CTL + 300, L_TAG = L_CTL + 512, C_X = L_CTL + 304, L_PROG = L_CTL + 320, C_END = L_CTL + 336, L_TOTAL = 163840;
constexpr u32 SPIN_CAP = 1u << 22;
#define UPST() do {} while (0)
#define OUTST 0
#define WBSEL (ii + w)
#define ESLEEP 3
#define ST1(id) do {} while (0)
#define TLBT 0
#define HP2 0
#define RMID 7
#define KVG 4
#define KVS 128
#define KVOFF(p) (((p) >> KVG) * KVS + ((p) & ((1 << KVG) - 1)) * 8)
#define WSEND 0
#define FS 128
#define GATE 1
#define TDELAY 0
#define TOUCH 0
constexpr float QSCALE = 0.25f * 1.4426950408889634f;

#define STAMP(id) do {} while (0)
#define STAMP_FLUSH(base) do {} while (0)

template <typename T> __device__ __forceinline__ AS3 T* lp(u32 a) { return (AS3 T*)(size_t)a; }
__device__ __forceinline__ u32x4 mksrd(const void* p, u32 bytes) {
    u32x4 s; s[0] = (u32)(size_t)p; s[1] = (u32)((size_t)p >> 32) & 0xffffu; s[2] = bytes; s[3] = 0x00020000u; return s; }
#define DPP_ADD(v, ctrl) ((v) + __builtin_bit_cast(float, __builtin_amdgcn_update_dpp(0, __builtin_bit_cast(int, (v)), (ctrl), 0xF, 0xF, true)))
__device__ __forceinline__ float sum8(float v) { v = DPP_ADD(v, 0xB1); v = DPP_ADD(v, 0x4E); v = DPP_ADD(v, 0x141); return v; }
__device__ __forceinline__ float sum32(float v) {
    v = sum8(v); v = DPP_ADD(v, 0x140);
    auto a = __builtin_amdgcn_permlane16_swap(__float_as_uint(v), __float_as_uint(v), false, false); return __uint_as_float(a[0]) + __uint_as_float(a[1]); }
__device__ __forceinline__ u32 pk(float lo, float hi) { bf16x2 r = {(__bf16)lo, (__bf16)hi}; return __builtin_bit_cast(u32, r); }
__device__ __forceinline__ u32 relu2(u32 v) {
    typedef short s16x2 __attribute__((ext_vector_type(2)));
    return __builtin_bit_cast(u32, __builtin_elementwise_max(__builtin_bit_cast(s16x2, v), s16x2{0, 0})); }
__device__ __forceinline__ float rbf(float v) { return (float)(__bf16)v; }
__device__ __forceinline__ f32x2 unpk(u32 v) { return f32x2{__uint_as_float(v << 16), __uint_as_float(v & 0xffff0000u)}; }
__device__ __forceinline__ float rstd_of(float sumsq, float dm) {
    float var = rbf(fmaxf(__builtin_fmaf(-dm, dm, sumsq * (1.f / 512.f)), 0.f));
    return __builtin_amdgcn_rcpf(rbf(__builtin_amdgcn_sqrtf(rbf(var + 1e-5f)))); }
__device__ __forceinline__ void lds_arrive(u32 a, int) {
    u64 t; asm volatile("s_mov_b64 %0, exec\n\ts_mov_b64 exec, 1\n\tds_add_u32 %1, %2\n\ts_mov_b64 exec, %0" : "=&s"(t) : "v"(a), "v"(1u) : "memory"); }
__device__ __forceinline__ u32 lds_arrive_rtn(u32 a, int lane) {
    MEMBAR(); u32 o = 0; if (lane == 0) o = __hip_atomic_fetch_add(lp<u32>(a), 1u, __ATOMIC_RELAXED, __HIP_MEMORY_SCOPE_WORKGROUP);
    o = RFL(o); MEMBAR(); return o; }
__device__ __forceinline__ void lds_wait(u32 a, u32 val) {
    u32 t, n;
    asm volatile("s_mov_b32 %1, 0\n1:\n\tds_read_b32 %0, %2\n\ts_waitcnt lgkmcnt(0)\n\tv_readfirstlane_b32 vcc_lo, %0\n\ts_add_u32 %1, %1, 1\n\t"
                 "s_cmp_ge_u32 vcc_lo, %3\n\ts_cbranch_scc1 2f\n\ts_sleep 1\n\ts_cmp_lt_u32 %1, 0x400000\n\ts_cbranch_scc1 1b\n2:"
                 : "=&v"(t), "=&s"(n) : "v"(a), "n"(val) : "memory", "scc", "vcc"); }
__device__ __forceinline__ void lds_wait0(u32 a, u32 val) {
    u32 t, n;
    asm volatile("s_mov_b32 %1, 0\n1:\n\tds_read_b32 %0, %2\n\ts_waitcnt lgkmcnt(0)\n\tv_readfirstlane_b32 vcc_lo, %0\n\ts_add_u32 %1, %1, 1\n\t"
                 "s_cmp_ge_u32 vcc_lo, %3\n\ts_cbranch_scc1 2f\n\ts_cmp_lt_u32 %1, 0x400000\n\ts_cbranch_scc1 1b\n2:"
                 : "=&v"(t), "=&s"(n) : "v"(a), "n"(val) : "memory", "scc", "vcc"); }
__device__ __forceinline__ u64 rd_tag(u32 a) { const u32x2 t = *lp<u32x2>(a); return ((u64)(u32)RFL(t[1]) << 32) | (u64)(u32)RFL(t[0]); }
__device__ __forceinline__ u64 ld_flag(const char* p) { return __hip_atomic_load((AS1 u64*)(size_t)p, __ATOMIC_RELAXED, __HIP_MEMORY_SCOPE_AGENT); }
__device__ __forceinline__ void st_flag(char* p, u64 v) { __hip_atomic_store((AS1 u64*)(size_t)p, v, __ATOMIC_RELAXED, __HIP_MEMORY_SCOPE_AGENT); }
__device__ __forceinline__ void wait_flags(const char* p, u64 tag) {
#pragma unroll 1
    for (u32 it = 0; it < SPIN_CAP; ++it) { if (__all(ld_flag(p) == tag)) break; __builtin_amdgcn_s_sleep(1); }
    __builtin_amdgcn_fence(__ATOMIC_ACQUIRE, "wavefront"); MEMBAR(); }
__device__ __forceinline__ bf16x8 trfrag(u32 a0, u32 a1) {
    s16x4 lo = __builtin_amdgcn_ds_read_tr16_b64_v4i16(lp<s16x4>(a0)), hi = __builtin_amdgcn_ds_read_tr16_b64_v4i16(lp<s16x4>(a1));
    return __builtin_bit_cast(bf16x8, __builtin_shufflevector(lo, hi, 0, 1, 2, 3, 4, 5, 6, 7)); }
#define MFMA32(a, b, c) __builtin_amdgcn_mfma_f32_32x32x16_bf16(a, b, c, 0, 0, 0)
#define MFMA16(a, b, c) __builtin_amdgcn_mfma_f32_16x16x32_bf16(a, b, c, 0, 0, 0)
#define GLDS(voff, srd, soff, ldsa) asm volatile("s_mov_b32 m0, %2\n\ts_nop 0\n\tbuffer_load_dwordx4 %0, %1, %3 offen lds" :: "v"(voff), "s"(srd), "s"(ldsa), "s"(soff) : "memory")
#define BLOAD4(dst, voff, srd, soff) asm volatile("buffer_load_dwordx4 %0, %1, %2, %3 offen" : "=v"(dst) : "v"(voff), "s"(srd), "s"(soff) : "memory")
#define AUX_SC1 16

__global__ __launch_bounds__(512, 2) void fused_block_kernel(const u16* __restrict__ x, const u16* __restrict__ wq, const u16* __restrict__ wk,
        const u16* __restrict__ wv, const u16* __restrict__ wo, const u16* __restrict__ w1, const u16* __restrict__ w2,
        u16* __restrict__ out, char* ws) {
    __shared__ __attribute__((aligned(1024))) char smem[L_TOTAL];
    const u32 L0 = (u32)(size_t)(AS3 char*)smem;
    const int lane = threadIdx.x & 63;
    const int w = RFL(threadIdx.x >> 6);
    const int b = blockIdx.x, xc = b & 7, ii = b >> 3;
    const int j = 4 * (xc & 1) + (ii & 3), g = 8 * (xc >> 1) + (ii >> 2), gl = ii >> 2, sib = ii & 3;
    const int hl = lane >> 5, l31 = lane & 31, l15 = lane & 15, g4 = lane >> 4;
    STAMP(0);
#define DG(i) do {} while (0)
    const u32x4 srdX = mksrd(x, 2048 * 512 * 2), srdWS = mksrd(ws, WS_END), srdWQ = mksrd(wq, 512 * 1024);
    const __amdgpu_buffer_rsrc_t rsWS = __builtin_amdgcn_make_buffer_rsrc((void*)ws, 0, (int)WS_END, 0x00020000);

    u32x2 ev; u32x4 own0, own1, xr[8]; u32 cpf;
    { const u32 z = 0, vo = hl * 1024 + l31 * 16, so = (64 * g + 8 * j + 2 * (w & 3)) * 1024;
      asm volatile("s_cmp_eq_u32 %7, 0\n\ts_cbranch_scc0 1f\n\tbuffer_load_dwordx2 %0, %3, %4, 0 offen sc1\n1:\n\t"
                   "s_cmp_lt_u32 %7, 4\n\ts_cbranch_scc0 2f\n\tbuffer_load_dwordx4 %1, %5, %6, %8 offen\n\tbuffer_load_dwordx4 %2, %5, %6, %8 offen offset:512\n2:"
                   : "=&v"(ev), "=&v"(own0), "=&v"(own1) : "v"(z), "s"(srdWS), "v"(vo), "s"(srdX), "s"(w), "s"(so) : "memory", "scc"); }
    if (w < 4) { const u32x4 srdKV = mksrd(w < 2 ? wk : wv, 16384); const u32 vo = lane * 16;
#pragma unroll
      for (int i = 0; i < 8; ++i) { const u32 so = (w & 1) * 8192 + i * 1024, la = L0 + L_WKV + w * 8192 + i * 1024; GLDS(vo, srdKV, so, la); } }
    if (w == 7 && lane < 32) *lp<u32>(L0 + C_LN + lane * 4) = 0;
    asm volatile("s_waitcnt lgkmcnt(0)\n\ts_barrier" ::: "memory");
    const int oct = (j + w) & 7, gi = w - 4;
    const u32 s0 = 4 * gl;
    const int ft0 = gi < 3 ? (j & 1) + 2 * gi : ((j & 1) ? 0 : 6);
    const u32x4 srdW1 = mksrd(w1, 512 * 3584);
    { const u32 vo = (lane >> 3) * 1024 + (lane & 7) * 16; u32 so[8]; u64 pc; u32 clen;
      asm volatile("s_getpc_b64 %0\n.Lcode_anchor:\n\ts_mov_b32 %1, .Lcode_tail-.Lcode_anchor" : "=s"(pc), "=s"(clen));
      const u32x4 srdT = mksrd((const void*)(pc & ~3ull), clen);
      const u32 vt = (ii & 7) * 8192 + lane * 128;
#pragma unroll
      for (int n = 0; n < 8; ++n) so[n] = (64 * g + 8 * oct) * 1024 + ((n + 2 * sib) & 7) * 128;
      asm volatile("s_cmp_eq_u32 %11, 0\n\ts_cbranch_scc1 1f\n\t"
                   "buffer_load_dwordx4 %0, %9, %10, %12 offen\n\tbuffer_load_dwordx4 %1, %9, %10, %13 offen\n\t"
                   "buffer_load_dwordx4 %2, %9, %10, %14 offen\n\tbuffer_load_dwordx4 %3, %9, %10, %15 offen\n\t"
                   "buffer_load_dwordx4 %4, %9, %10, %16 offen\n\tbuffer_load_dwordx4 %5, %9, %10, %17 offen\n\t"
                   "buffer_load_dwordx4 %6, %9, %10, %18 offen\n\tbuffer_load_dwordx4 %7, %9, %10, %19 offen\n\t"
                   "s_branch 2f\n1:\n\t"
                   "buffer_load_dword %8, %20, %21, 0 offen\n2:"
                   : "=&v"(xr[0]), "=&v"(xr[1]), "=&v"(xr[2]), "=&v"(xr[3]), "=&v"(xr[4]), "=&v"(xr[5]), "=&v"(xr[6]), "=&v"(xr[7]), "=&v"(cpf)
                   : "v"(vo), "s"(srdX), "s"(w), "s"(so[0]), "s"(so[1]), "s"(so[2]), "s"(so[3]), "s"(so[4]), "s"(so[5]), "s"(so[6]), "s"(so[7]),
                     "v"(vt), "s"(srdT)
                   : "memory", "scc"); }
    const u32 swz8 = (lane >> 3) * 1024 + (((lane & 7) ^ (((lane >> 4) & 1) << 2)) * 16) + 128 * j;
    const u32 reg = L0 + L_GR + (w & 3) * 8192;
    const u32 vw1 = (gi < 3 ? (lane >> 3) * 3584 + (((lane & 7) ^ (((lane >> 4) & 1) << 2)) * 16) : (lane >> 2) * 3584 + (((lane & 3) ^ (hl << 1)) * 16)) + 448 * j + 64 * ft0;
    const u32 vw1b = (lane >> 3) * 3584 + (((lane & 7) ^ (((lane >> 4) & 1) << 2) ^ 2) * 16) + 448 * j + 64 * ft0;
    auto wq_piece = [&](u32 pi) { const u32 n = pi >> 1, so = ((s0 + n) & 31) * 16384 + (pi & 1) * 8192;
        const u32 la = pi < 62 ? L0 + L_WQ + pi * 1024 : L0 + L_WQL + (pi - 62) * 1024; GLDS(swz8, srdWQ, so, la); };

    asm volatile("s_cmp_eq_u32 %3, 0\n\ts_cbranch_scc1 1f\n\ts_waitcnt vmcnt(16)\n\ts_branch 2f\n1:\n\ts_waitcnt vmcnt(9)\n2:"
                 : "+v"(own0), "+v"(own1), "+v"(ev) : "s"(w) : "scc");
    STAMP(1);
    if (w < 4) {
      __builtin_amdgcn_s_setprio(2);
      f32x2 u[8];
#pragma unroll
      for (int i = 0; i < 4; ++i) { u[i] = unpk(own0[i]); u[4 + i] = unpk(own1[i]); }
      const f32x2 s = ((u[0] + u[1]) + (u[2] + u[3])) + ((u[4] + u[5]) + (u[6] + u[7]));
      const float m = sum32(s[0] + s[1]) * (1.f / 512.f), mb = rbf(m);
      u32x4 d0, d1; f32x2 q = {0.f, 0.f}, q2 = {0.f, 0.f};
#pragma unroll
      for (int i = 0; i < 4; ++i) { u[i] -= mb; u[4 + i] -= mb; d0[i] = pk(u[i][0], u[i][1]); d1[i] = pk(u[4 + i][0], u[4 + i][1]); }
      const u32 ad = L0 + L_XN + (8 * j + 2 * w + hl) * XROW + l31 * 16;
      *lp<u32x4>(ad) = d0; *lp<u32x4>(ad + 512) = d1;
#pragma unroll
      for (int i = 0; i < 4; ++i) { q = __builtin_elementwise_fma(u[i], u[i], q); q2 = __builtin_elementwise_fma(u[4 + i], u[4 + i], q2); }
      q += q2;
      const float rs = rstd_of(sum32(q[0] + q[1]), m - mb);
      if (l31 == 0) *lp<float>(L0 + L_RSTD + (8 * j + 2 * w + hl) * 4) = rs;
      asm volatile("s_cmp_eq_u32 %0, 0\n\ts_cbranch_scc1 1f\n\ts_waitcnt vmcnt(8)\n\ts_branch 2f\n1:\n\ts_waitcnt vmcnt(1)\n2:" :: "s"(w) : "memory", "scc");
      if (w != 0) { lds_arrive(L0 + C_OWN, lane); __builtin_amdgcn_s_setprio(0); }
    }
    if (w == 0) {
        STAMP(2);
        const u64 tag0 = (((u64)(u32)RFL(ev[1]) << 32) | (u64)(u32)RFL(ev[0])) + 1;
        *lp<u64>(L0 + L_TAG) = tag0;
        f32x4 ka, va; u32 t0;
        asm volatile(ASM_KV : "=&v"(ka), "=&v"(va), "=&s"(t0)
                     : "v"(L0 + L_WKV + (8 * g4 + (l15 >> 2)) * 32 + (lane & 3) * 8), "v"(L0 + L_XN + (8 * j + (lane & 7)) * XROW + g4 * 16), "s"(L0 + C_OWN)
                     : "memory", "scc", "vcc", CLOB_KV);
        lds_arrive(L0 + C_KV, lane);
        STAMP(8);
        const float rk = *lp<float>(L0 + L_RSTD + (8 * j + (lane & 7)) * 4);
        const f32x4 rv = *lp<f32x4>(L0 + L_RSTD + (8 * j + 4 * (g4 & 1)) * 4);
        const int p = 8 * g + j, key = 8 * p + l15;
        u32x2 kp = {pk(ka[0] * rk, ka[1] * rk), pk(ka[2] * rk, ka[3] * rk)};
        const u32x2 vp = {pk(va[0] * rv[0], va[1] * rv[1]), pk(va[2] * rv[2], va[3] * rv[3])};
        const u32 ko = l15 < 8 ? WS_K + (key >> 5) * 1024 + ((g4 & 1) * 32 + (key & 31)) * 16 + (g4 >> 1) * 8 : 0x80000000u;
        const u32 vo = g4 < 2 ? WS_V + (p >> 2) * 1024 + (p & 1) * 512 + ((p >> 1) & 1) * 256 + l15 * 16 + g4 * 8 : 0x80000000u;
        __builtin_amdgcn_raw_buffer_store_b64(kp, rsWS, ko, 0, AUX_SC1);
        __builtin_amdgcn_raw_buffer_store_b64(vp, rsWS, vo, 0, AUX_SC1);
        STAMP(9);
        asm volatile("s_waitcnt vmcnt(0)" : "+v"(cpf) :: "memory");
        if (lane == 0) st_flag(ws + WS_KVF + KVOFF(p), tag0);
        STAMP(3);
    } else {
        STAMP(2);
#pragma unroll
        for (int m = 0; m < 4; ++m) wq_piece((u32)(w - 1) + 7u * m);
        f32x2 u[32], sa = {0.f, 0.f}, sb = {0.f, 0.f};
#pragma unroll
        for (int n = 0; n < 8; n += 2) {
            asm volatile("s_waitcnt vmcnt(%2)" : "+v"(xr[n]), "+v"(xr[n + 1]) : "n"(4 + 6 - n));
#pragma unroll
            for (int i = 0; i < 4; ++i) { u[4 * n + i] = unpk(xr[n][i]); u[4 * n + 4 + i] = unpk(xr[n + 1][i]); sa += u[4 * n + i]; sb += u[4 * n + 4 + i]; } }
        sa += sb;
        STAMP(7);
        const float m = sum8(sa[0] + sa[1]) * (1.f / 512.f), mb = rbf(m);
        const int row = 8 * oct + (lane >> 3);
        const u32 ad = L0 + L_XN + row * XROW + (lane & 7) * 16;
        auto ring0 = [&]() {
            if (w < 7) {
#pragma unroll
                for (int m2 = 0; m2 < 4; ++m2) { const u32 so = (s0 + m2) * 57344, la = reg + m2 * 2048; GLDS(vw1, srdW1, so, la); GLDS(vw1b, srdW1, so + 28672, la + 1024); }
            } else {
#pragma unroll
                for (int m2 = 0; m2 < 4; ++m2) { const u32 so = (s0 + m2) * 57344, la = reg + m2 * 1024; GLDS(vw1, srdW1, so, la); } } };
        bool rdone = false;
#pragma unroll
        for (int n = 0; n < 8; ++n) { u32x4 d;
#pragma unroll
            for (int i = 0; i < 4; ++i) { u[4 * n + i] -= mb; d[i] = pk(u[4 * n + i][0], u[4 * n + i][1]); }
            *lp<u32x4>(ad + ((n + 2 * sib) & 7) * 128) = d;
            if (n == 2) { if (w < 4) asm volatile("s_waitcnt vmcnt(2)" ::: "memory"); else asm volatile("s_waitcnt vmcnt(0)" ::: "memory");
                lds_arrive(L0 + C_WQ, lane); }
            if (w < 4) wq_piece(27u + w + 3u * n);
            else if (n == RMID) { if (RFL(*(volatile AS3 u32*)(size_t)(L0 + C_KV)) >= 1) { ring0(); rdone = true; } } }
        lds_arrive(L0 + C_LN, lane);
        STAMP(8);
        if (!rdone) { lds_wait(L0 + C_KV, 1);
            if (w < 4) { wq_piece(51u + w); wq_piece(54u + w); wq_piece(57u + w); wq_piece(60u + w); }
            else ring0(); }
        f32x2 qa = {0.f, 0.f}, qb = {0.f, 0.f};
#pragma unroll
        for (int n = 0; n < 32; n += 2) { qa = __builtin_elementwise_fma(u[n], u[n], qa); qb = __builtin_elementwise_fma(u[n + 1], u[n + 1], qb); }
        qa += qb;
        const float rs = rstd_of(sum8(qa[0] + qa[1]), m - mb);
        if ((lane & 7) == 0) *lp<float>(L0 + L_RSTD + row * 4) = rs;
        lds_arrive(L0 + C_RS, lane);
        STAMP(3);
    }
    const u32 q4 = l15 >> 2, trs = (q4 >> 1) << 6;

    auto part = [&](int qt, int ln, f32x4& oa, f32x4& ob, float& l) {
        const u32 pb = L0 + (qt < 6 ? L_P0 + qt * 4 * PSLOT : L_P6 + (qt - 6) * 4 * PSLOT);
        f32x4 a[4], c[4]; float e[4];
#pragma unroll
        for (int s = 0; s < 4; ++s) { e[s] = *lp<float>(pb + s * PSLOT + 2048 + (ln & 31) * 4); a[s] = *lp<f32x4>(pb + s * PSLOT + ln * 16); c[s] = *lp<f32x4>(pb + s * PSLOT + 1024 + ln * 16); }
        asm volatile("" : "+v"(a[0]), "+v"(a[1]), "+v"(a[2]), "+v"(a[3]), "+v"(c[0]), "+v"(c[1]), "+v"(c[2]), "+v"(c[3]), "+v"(e[0]), "+v"(e[1]), "+v"(e[2]), "+v"(e[3]));
        oa = (a[0] + a[1]) + (a[2] + a[3]); ob = (c[0] + c[1]) + (c[2] + c[3]); l = (e[0] + e[1]) + (e[2] + e[3]); };
    auto redo = [&](int qt, int ln, f32x4& oa, f32x4& ob, float& l) {
        const bf16x8 qf = *lp<bf16x8>(L0 + L_Q + qt * 1024 + ln * 16);
        const f32x16 zero = {};
        float mx = -__builtin_inff();
#pragma unroll 1
        for (int t = 0; t < 64; ++t) {
            const f32x16 s = MFMA32(__builtin_bit_cast(bf16x8, __builtin_amdgcn_raw_buffer_load_b128(rsWS, ln * 16, WS_K + t * 1024, AUX_SC1)), qf, zero);
#pragma unroll
            for (int r = 0; r < 16; ++r) mx = fmaxf(mx, s[r]); }
        mx = fmaxf(mx, __shfl_xor(mx, 32));
        f32x16 acc = {};
        const u32 pad = (ln & 15) == 0 ? 0x3F803F80u : 0u;
#pragma unroll 1
        for (int t = 0; t < 64; ++t) {
            f32x16 s = MFMA32(__builtin_bit_cast(bf16x8, __builtin_amdgcn_raw_buffer_load_b128(rsWS, ln * 16, WS_K + t * 1024, AUX_SC1)), qf, zero);
#pragma unroll
            for (int r = 0; r < 16; ++r) s[r] = __builtin_amdgcn_exp2f(s[r] - mx);
#pragma unroll
            for (int u = 0; u < 2; ++u) {
                u32x4 vf = __builtin_amdgcn_raw_buffer_load_b128(rsWS, (ln >> 5) * 512 + u * 256 + (ln & 15) * 16, WS_V + t * 1024, AUX_SC1);
                if (ln & 16) vf = u32x4{pad, pad, pad, pad};
                const f32x8 ph = u ? __builtin_shufflevector(s, s, 8, 9, 10, 11, 12, 13, 14, 15) : __builtin_shufflevector(s, s, 0, 1, 2, 3, 4, 5, 6, 7);
                acc = __builtin_amdgcn_smfmac_f32_32x32x32_bf16(__builtin_bit_cast(bf16x8, vf), __builtin_bit_cast(bf16x16, ph), acc, 0xDDDD, 0, 0); } }
        oa = f32x4{acc[0], acc[1], acc[2], acc[3]}; ob = f32x4{acc[4], acc[5], acc[6], acc[7]};
        l = __shfl(acc[8], ln & 31); };
    auto safe = [&](float l) -> bool { return __all(l >= 0x1p-80f && l <= 0x1p80f); };
    auto norm = [&](const f32x4& oa, const f32x4& ob, float l) -> u32x4 { const float rl = __builtin_amdgcn_rcpf(l);
        return u32x4{pk(oa[0] * rl, oa[1] * rl), pk(oa[2] * rl, oa[3] * rl), pk(ob[0] * rl, ob[1] * rl), pk(ob[2] * rl, ob[3] * rl)}; };
    auto emit_tagged = [&](int hp, int ln, const u32x4& o, u32 t) { const u32 so = WS_T + (hp * 256 + g * 8 + j) * 2048;
        __builtin_amdgcn_raw_buffer_store_b128(u32x4{o[0], t, o[1], t}, rsWS, ln * 16, so, AUX_SC1);
        __builtin_amdgcn_raw_buffer_store_b128(u32x4{o[2], t, o[3], t}, rsWS, ln * 16 + 1024, so, AUX_SC1); };

    if (w < 4) {
        __builtin_amdgcn_s_setprio(0);
        int ln; asm volatile("v_mbcnt_lo_u32_b32 %0, -1, 0\n\tv_mbcnt_hi_u32_b32 %0, -1, %0" : "=v"(ln));
        const int lane = ln, hl = ln >> 5, l31 = ln & 31, l15 = ln & 15, g4 = ln >> 4;
        const u32 q4 = l15 >> 2, trl = (g4 & 1) * 32 + (lane & 3) * 8, trs = (q4 >> 1) << 6;
        const int c = w & 1, tt = w >> 1;
        const u32 va = (8 * hl + q4) * 128 + ((c * 64 + trl) ^ trs);
        u32 vk = lane * 16, t0, t1, t2;
        asm volatile(ASM_ATTN : "=&s"(t0), "=&s"(t1), "=&s"(t2), "+{v1}"(vk)
                     : "s"(srdWS), "s"(WS_K + w * 16384), "s"(WS_V + w * 16384), "s"(L0 + C_LN), "s"(s0), "s"(ii & 15), "s"(WS_T + (256 + g * 8 + j) * 2048),
                       "{v2}"(l15 == 0 ? 0x3F803F80u : 0u), "{v3}"(L0 + L_P0 + w * PSLOT + lane * 4),
                       "{v196}"(L0 + L_WQ + va), "{v197}"(L0 + L_WQL + va), "{v198}"(L0 + L_XN + (32 * tt + l31) * XROW + hl * 16),
                       "{v199}"(L0 + L_RSTD + (32 * tt + l31) * 4), "{v200}"(L0 + L_Q + (2 * (2 * tt + (l31 >> 4)) + c) * 1024 + (l15 + 32 * hl) * 16),
                       "{v201}"(WS_KVF + KVOFF(64 * w + lane)),
                       "{v253}"(L0 + L_Q + lane * 16), "{v254}"(L0 + L_P0 + w * PSLOT + lane * 16), "{v255}"(L0 + L_PROG + w * 4)
                     : "memory", "scc", "vcc", "s60", "s61", "s63", "s64", "s65", "s66", "s67", "s68", "s69", "s70", CLOB_ATTN);
        if (__builtin_expect(t0 != 0, 0)) {
            const int ln = __lane_id(); f32x4 oa, ob; float l;
            redo(7, ln, oa, ob, l);
            emit_tagged(1, ln, norm(oa, ob, l), *lp<u32>(L0 + L_TAG));
        }
        return;
    }

    u64 tag;
    f32x32 c0, c1; u32x32 wo1;
    const u32x4 srdWO = mksrd(wo, 512 * 1024);
    const u32 vwo = (lane >> 3) * 1024 + (lane & 7) * 16 + 128 * j;
    const u32 awl = (16 * (g4 & 1) + 4 * (g4 >> 1) + q4) * 128 + (lane & 3) * 8, aw = reg + awl;
    const u32x4 srdW2 = mksrd(w2, 1792 * 1024);
    const u32 vW2 = (lane >> 3) * 1024 + (((lane & 7) ^ (((lane >> 4) & 1) << 2) ^ (hl << 1)) * 16) + 128 * j;
    const u32 vA2 = (4 * g4 + q4) * 128 + (((lane & 3) * 8) ^ trs ^ ((g4 & 1) << 5));
    { const u32 vB = L0 + L_XN + l15 * XROW + g4 * 16, rx = L0 + L_UX + gi * 8192;
      u32 t0, t1;
#define UP_CALL(TEXT, VB2, VA) asm volatile(TEXT : "=&{v[64:95]}"(c0), "=&{v[96:127]}"(c1), "=&{v[32:63]}"(wo1), "=&s"(t0), "=&s"(t1) \
          : "s"(srdW1), "s"(s0), "v"(vw1), "v"(VB2), "s"(reg), "s"(rx), "v"(VA), "v"(vB), "s"(L0 + C_LN), \
            "s"(srdW2), "v"(vW2), "s"(7 * j + ft0), "s"(srdWS), "v"(lane * 16), "s"(WS_H + (g * 56 + 7 * j + ft0) * 4096), "v"(vA2), "s"(gi), \
            "s"(srdWO), "v"(vwo), "v"(rx + awl) \
          : "memory", "scc", "vcc", CLOB_UP)
      UP_CALL(ASM_UP, vw1b, gi < 3 ? (8 * g4 + q4) * 128 + (((lane & 3) * 8) ^ trs ^ ((g4 & 1) << 5)) : (8 * g4 + q4) * 64 + (((lane & 3) * 8) ^ ((g4 & 1) << 5)));
      STAMP(4);
      DG(0); }
    tag = rd_tag(L0 + L_TAG);
    asm volatile("s_waitcnt vmcnt(0)" ::: "memory");
    if (lds_arrive_rtn(L0 + C_H, lane) == 3) { if (lane == 0) st_flag(ws + WS_HF + (g * 8 + j) * FS, tag); }
    STAMP(5);

    lds_wait(L0 + C_H, 4);
    STAMP(6);
    u32x32 wo2;
    { const u32 nst = gi < 3 ? 12 : 13;
      u32 sidx = (((gl + 2 * sib) & 7) * nst) >> 3, t0, t1, t2;
      const u32x4 srdH = mksrd(ws + WS_H + g * 56 * 4096, 56 * 4096);
#define DOWN_CALL(TEXT) asm volatile(TEXT : "+{v[64:95]}"(c0), "+{v[96:127]}"(c1), "=&{v[204:235]}"(wo2), "+&s"(sidx), "=&s"(t0), "=&s"(t1), "=&s"(t2) \
          : "s"(srdW2), "s"(srdH), "s"(12 * gi), "s"(reg), "s"(L0 + L_R2 + gi * 8192), "v"(vW2), "v"(lane * 16), "v"(vA2), "s"(gi), \
            "s"(srdWS), "v"(WS_HF + (g * 8 + (lane & 7)) * FS), "s"(tag), "s"(srdWO), "v"(vwo), "s"((128 * gi + 64) * 1024), "v"(aw), "s"(7 * j) \
          : "memory", "scc", "vcc", "s64", "s65", CLOB_DOWN)
      DOWN_CALL(ASM_DOWN); }
    STAMP(15); DG(1);
    { int ln2; asm volatile("v_mbcnt_lo_u32_b32 %0, -1, 0\n\tv_mbcnt_hi_u32_b32 %0, -1, %0" : "=v"(ln2));
    const int lane = ln2, l15 = ln2 & 15, g4 = ln2 >> 4;
    auto prog = [&]() -> u32 { const u32x4 pr = *(volatile AS3 u32x4*)(size_t)(L0 + L_PROG); const u32 mn = RFL(min(min(pr[0], pr[1]), min(pr[2], pr[3]))); MEMBAR(); return mn; };
    auto prog_wait = [&](u32 n) {
#pragma unroll 1
        for (u32 it = 0; it < SPIN_CAP; ++it) { if (prog() >= n) break; __builtin_amdgcn_s_sleep(2); } };
    int pend = -1;
    auto put_tile = [&](int qt, const u32x4& o) {
        if (qt < 6) { __builtin_amdgcn_raw_buffer_store_b128(o, rsWS, lane * 16, WS_O + ((g * 4 + (qt >> 1)) * 16 + 2 * j + (qt & 1)) * 1024, AUX_SC1); pend = qt; }
        else emit_tagged(0, lane, o, (u32)tag); };
    auto finish = [&](int qt) { f32x4 oa, ob; float l;
        prog_wait(qt + 1); part(qt, lane, oa, ob, l);
        if (__builtin_expect(!safe(l), 0)) redo(qt, lane, oa, ob, l);
        put_tile(qt, norm(oa, ob, l)); };
    auto publish = [&]() {
        if (pend >= 0) { if (lane == 0) st_flag(ws + WS_OF + ((g * 8 + pend) * 8 + j) * FS, tag); pend = -1; STAMP(10); } };
    u32x2 xe[4];
#pragma unroll
    for (int rb = 0; rb < 4; ++rb) xe[rb] = *(const u32x2*)(x + (64 * g + 16 * rb + l15) * 512 + 64 * j + 16 * gi + 4 * g4);
    finish(gi);
    u32x2 pf; const u32 vfa = WS_OF + ((g * 8 + min(lane >> 1, 5)) * 8 + 2 * gi + (lane & 1)) * FS;
    u32 vfl = (lane < 8 || !GATE || prog() > 5) ? vfa : 0x80000000u;
#define POLL_ISSUE() asm volatile("buffer_load_dwordx2 %0, %1, %2, 0 offen sc1" : "=v"(pf) : "v"(vfl), "s"(srdWS) : "memory")
    auto poll_wait = [&](u32 mask) -> u32 { u32 got = 0;
#pragma unroll 1
        for (u32 it = 0;; ++it) {
            asm volatile("s_waitcnt vmcnt(0)" : "+v"(pf) :: "memory");
            got = (u32)__ballot(pf[0] == (u32)tag && pf[1] == (u32)(tag >> 32));
            if ((got & mask) == mask || it >= SPIN_CAP) break;
            publish();
            __builtin_amdgcn_s_sleep(1);
            POLL_ISSUE(); }
        return got; };
    f32x4 sg[4];
    { auto slab = [&](int src, int d) -> u32 { return (d < 3 ? L0 + L_GR + src * 8192 + (d - 1) * 4096 : L0 + L_R2 + src * 8192) + lane * 16; };
#pragma unroll
      for (int d = 1; d < 4; ++d) { const u32 a = slab(gi, d);
#pragma unroll
          for (int rb = 0; rb < 4; ++rb) { const int o = 16 * (d & 1) + 4 * rb;
              *lp<f32x4>(a + rb * 1024) = d < 2 ? f32x4{c0[o], c0[o + 1], c0[o + 2], c0[o + 3]} : f32x4{c1[o], c1[o + 1], c1[o + 2], c1[o + 3]}; } }
      lds_arrive(L0 + C_R1, lane);
      asm volatile("s_waitcnt vmcnt(0)" ::: "memory"); publish();
      lds_wait(L0 + C_R1, 4);
      f32x4 t[12]; float rs[4];
#pragma unroll
      for (int d = 1; d < 4; ++d) { const u32 a = slab((gi - d) & 3, d);
#pragma unroll
          for (int rb = 0; rb < 4; ++rb) t[4 * (d - 1) + rb] = *lp<f32x4>(a + rb * 1024); }
#pragma unroll
      for (int rb = 0; rb < 4; ++rb) rs[rb] = *lp<float>(L0 + L_RSTD + (16 * rb + l15) * 4);
      lds_arrive(L0 + C_R2, lane);
      POLL_ISSUE();
      asm volatile("" : "+v"(t[0]), "+v"(t[1]), "+v"(t[2]), "+v"(t[3]), "+v"(t[4]), "+v"(t[5]), "+v"(t[6]), "+v"(t[7]), "+v"(t[8]), "+v"(t[9]), "+v"(t[10]), "+v"(t[11]),
                        "+v"(rs[0]), "+v"(rs[1]), "+v"(rs[2]), "+v"(rs[3]));
#pragma unroll
      for (int rb = 0; rb < 4; ++rb) {
          const f32x4 gv = ((f32x4{c0[4 * rb], c0[4 * rb + 1], c0[4 * rb + 2], c0[4 * rb + 3]} + t[rb]) + (t[4 + rb] + t[8 + rb])) * rs[rb];
          const f32x4 ge = gv * -1.4426950408889634f;
#pragma unroll
          for (int i = 0; i < 4; ++i) sg[rb][i] = gv[i] * __builtin_amdgcn_rcpf(1.f + __builtin_amdgcn_exp2f(ge[i])); } }
    STAMP(9);
    auto wfrag = [&](int f) -> bf16x8 { const int o = 4 * (f & 7);
        return __builtin_bit_cast(bf16x8, f < 8 ? u32x4{wo1[o], wo1[o + 1], wo1[o + 2], wo1[o + 3]} : u32x4{wo2[o], wo2[o + 1], wo2[o + 2], wo2[o + 3]}); };
    auto oload = [&](int rb, bf16x8 (&of)[4]) {
#pragma unroll
        for (int s = 0; s < 4; ++s) of[s] = __builtin_bit_cast(bf16x8, __builtin_amdgcn_raw_buffer_load_b128(rsWS, lane * 16 + s * 1024, WS_O + ((g * 4 + rb) * 16 + 4 * gi) * 1024, AUX_SC1)); };
    auto xout = [&](int rb, const f32x4 (&sv)[4]) {
        const u32 xb = (rb & 1) * 4096 + (rb >> 1) * (L_R2 - L_GR) + lane * 16;
#pragma unroll
        for (int k = 1; k < 4; ++k) *lp<f32x4>(reg + xb + (k - 1) * 1024) = sv[k];
        lds_arrive(L0 + C_X + rb * 4, lane); };
    auto X = [&](int rb, const bf16x8 (&of)[4]) -> f32x4 { f32x4 sv[4] = {};
#pragma unroll
        for (int s = 0; s < 4; ++s)
#pragma unroll
            for (int k = 0; k < 4; ++k) sv[k] = MFMA16(wfrag(4 * s + k), of[s], sv[k]);
        xout(rb, sv); return sv[0]; };
    auto Y = [&](int rb, const f32x4& own) {
        lds_wait0(L0 + C_X + rb * 4, 4);
        const int tok = 16 * rb + l15;
        const u32 xb = (rb & 1) * 4096 + (rb >> 1) * (L_R2 - L_GR) + lane * 16;
        f32x4 t[3];
#pragma unroll
        for (int d = 1; d < 4; ++d) t[d - 1] = *lp<f32x4>(L0 + L_GR + ((gi + d) & 3) * 8192 + xb + (3 - d) * 1024);
        asm volatile("" : "+v"(t[0]), "+v"(t[1]), "+v"(t[2]));
        const f32x4 mine = (own + t[0]) + (t[1] + t[2]);
        float r[4];
#pragma unroll
        for (int i = 0; i < 4; ++i) { const u32 xu = (i & 1) ? (xe[rb][i >> 1] & 0xffff0000u) : (xe[rb][i >> 1] << 16);
            r[i] = __builtin_fmaf(mine[i], sg[rb][i], __uint_as_float(xu)); }
        *(u32x2*)(out + (64 * g + tok) * 512 + 64 * j + 16 * gi + 4 * g4) = u32x2{pk(r[0], r[1]), pk(r[2], r[3])}; };
    u32x16 ta, tb;
    const u32 sta = WS_T + (g * 8 + 2 * gi) * 2048, stb = sta + 256 * 2048, vt16 = lane * 16;
#define TAG_OP(TEXT, C, T, SO) asm volatile(TEXT : C(T) : "v"(vt16), "s"(srdWS), "s"(SO), "s"((u32)tag) : "memory", "scc", "vcc", "s64", "s65", "s66", "v160", "v161")
    auto tag_mma = [&](const u32x16& T, int hp, f32x4 (&sv)[4]) {
#pragma unroll
        for (int s = 0; s < 2; ++s) { const bf16x8 of = __builtin_bit_cast(bf16x8, u32x4{T[8 * s], T[8 * s + 2], T[8 * s + 4], T[8 * s + 6]});
#pragma unroll
            for (int k = 0; k < 4; ++k) sv[k] = MFMA16(wfrag(8 * s + 4 * hp + k), of, sv[k]); } };
    const int t2 = gi == 3 ? 6 : gi + 4;
    bool did2 = gi == 2;
    auto try2 = [&]() {
        if (!did2 && prog() > (u32)t2) { f32x4 oa, ob; float l; part(t2, lane, oa, ob, l);
            if (safe(l)) { put_tile(t2, norm(oa, ob, l)); did2 = true; } } };
    bf16x8 oa4[4], ob4[4], oc4[4];
    try2();
    const bool got2 = (poll_wait(0xFF) & 0xF00) == 0xF00;
    ST1(1);
    oload(0, oa4); oload(1, ob4);
    if (got2) oload(2, oc4);
    publish();
    try2();
    if (pend >= 0) { asm volatile("s_waitcnt vmcnt(0)" ::: "memory"); publish(); }
    lds_wait(L0 + C_R2, 4);
    const f32x4 sm0 = X(0, oa4), sm1 = X(1, ob4);
    ST1(2);
    if (gi == 3) try2();
    else if (!did2) { finish(t2); did2 = true; asm volatile("s_waitcnt vmcnt(0)" ::: "memory"); publish(); }
    if (!got2) { vfl = vfa; prog_wait(6); POLL_ISSUE(); Y(0, sm0); Y(1, sm1); if (TRY3 == 1) try2(); poll_wait(0xF00); oload(2, oc4); if (TRY3 == 2) try2(); }
    ST1(3);
    const f32x4 sm2 = X(2, oc4);
    ST1(4);
    if (!did2) finish(6);
    prog_wait(7);
    TAG_OP(ASM_TAGI_A, "=&{v[128:143]}", ta, sta);
    if (got2) { Y(0, sm0); Y(1, sm1); }
    STAMP(12);
    Y(2, sm2); STAMP(13);
    f32x4 sv[4] = {};
    TAG_OP(ASM_TAGSPIN_A, "+{v[128:143]}", ta, sta);
    tag_mma(ta, 0, sv);
    ST1(5);
    lds_wait(L0 + C_END, 4);
    if (TDELAY) __builtin_amdgcn_s_sleep(TDELAY);
    TAG_OP(ASM_TAGI_B, "=&{v[144:159]}", tb, stb);
    TAG_OP(ASM_TAGSPIN_B, "+{v[144:159]}", tb, stb);
    ST1(6);
    tag_mma(tb, 1, sv);
    xout(3, sv); ST1(8);
    Y(3, sv[0]); STAMP(14); }
    if (b == 0 && gi == 0 && __lane_id() == 0) st_flag(ws + WS_E, tag);
    STAMP_FLUSH(0);
}

extern "C" void kernel_launch(void* const* d_in, const int* in_sizes, int n_in, void* d_out, int out_size, void* d_ws, size_t ws_size, hipStream_t stream) {
    fused_block_kernel<<<256, 512, 0, stream>>>((const u16*)d_in[0], (const u16*)d_in[1], (const u16*)d_in[2], (const u16*)d_in[3], (const u16*)d_in[4],
                                               (const u16*)d_in[6], (const u16*)d_in[7], (u16*)d_out, (char*)d_ws + (WSEND ? ((ws_size - WSEND * 1048576ull) & ~4095ull) : 0));
}
```
